```python
import torch

def _softmax(x, axis=-1):
    return torch.softmax(x, dim=axis)
def _gelu(x):
    return torch.nn.functional.gelu(x, approximate="tanh")
def _silu(x):
    return torch.nn.functional.silu(x)
def _sigmoid(x):
    return torch.sigmoid(x)
def _layernorm(x, axis=-1, eps=1e-5):
    m = x.mean(dim=axis, keepdim=True)
    v = x.var(dim=axis, keepdim=True, unbiased=False)
    return (x - m) / torch.sqrt(v + eps)
def _scatter_add(tgt, idx, src, axis=0):
    return tgt.clone().index_add_(axis, idx, src)
def _segment_sum(src, seg, n):
    out = torch.zeros((n,) + src.shape[1:], dtype=src.dtype)
    return out.index_add_(0, seg, src)
def _pad(x, pw):
    flat = tuple(v for pair in reversed(pw) for v in pair)
    return torch.nn.functional.pad(x, flat)

kernel_name = 'interleave_layers__sliding_local_dilated__grouped_kv_mqa__prompt'

def setup_inputs():

    _w = (lambda a: (lambda t: t.to(torch.bfloat16) if t.is_floating_point() else t)(torch.as_tensor(a)))
    def _A_setup():
        B, S, D = 1, 2048, 576
        return {
            "x": _w((lambda *s: torch.randn(*s, dtype=torch.bfloat16) * 0.05)(B, S, D)),
            "wqkv": _w((lambda *s: torch.randn(*s, dtype=torch.bfloat16) * 0.05)(D, 3 * D)),
            "wo": _w((lambda *s: torch.randn(*s, dtype=torch.bfloat16) * 0.05)(D, D)),
        }
    _A_in = _A_setup()

    def _B_setup():
        B, S, D, H = 1, 2048, 576, 32
        Dh = D // H
        return {
            "x": _w((lambda *s: torch.randn(*s, dtype=torch.bfloat16) * 0.05)(B, S, D)),
            "wq": _w((lambda *s: torch.randn(*s, dtype=torch.bfloat16) * 0.05)(D, H * Dh)),
            "wk": _w((lambda *s: torch.randn(*s, dtype=torch.bfloat16) * 0.05)(D, Dh)),
            "wv": _w((lambda *s: torch.randn(*s, dtype=torch.bfloat16) * 0.05)(D, Dh)),
            "wo": _w((lambda *s: torch.randn(*s, dtype=torch.bfloat16) * 0.05)(H * Dh, D)),
        }
    _B_in = _B_setup()

    return {
        "A_x": _A_in["x"],
        "A_wqkv": _A_in["wqkv"],
        "A_wo": _A_in["wo"],
        "B_x": _B_in["x"],
        "B_wq": _B_in["wq"],
        "B_wk": _B_in["wk"],
        "B_wv": _B_in["wv"],
        "B_wo": _B_in["wo"],
    }

def reference(A_x, A_wqkv, A_wo, B_x, B_wq, B_wk, B_wv, B_wo):
    def _A_ref(x, wqkv, wo):
        B, S, D = x.shape
        H = 32; Dh = D // H
        W, dil = 8, 2
        qkv = (lambda x, *d: x.permute(*d))((x @ wqkv).reshape(B, S, 3, H, Dh), 2, 0, 3, 1, 4)
        q, k, v = qkv[0], qkv[1], qkv[2]
        scale = Dh ** -0.5
        pos = torch.arange(S)
        off = torch.arange(W) * dil
        idx = pos[:, None] - off[None, :]
        mask = idx >= 0
        idx_c = torch.where(mask, idx, 0).reshape(-1)
        kg = k[:, :, idx_c, :].reshape(B, H, S, W, Dh)
        vg = v[:, :, idx_c, :].reshape(B, H, S, W, Dh)
        logits = torch.einsum("bhsd,bhswd->bhsw", q, kg) * scale
        logits = torch.where(mask[None, None, :, :], logits, -1e9)
        attn = _softmax(logits, axis=-1)
        o = torch.einsum("bhsw,bhswd->bhsd", attn, vg)
        return (lambda x, *d: x.permute(*d))(o, 0, 2, 1, 3).reshape(B, S, D) @ wo

    def _B_ref(x, wq, wk, wv, wo):
        B, S, D = x.shape
        H = 32
        Dh = D // H
        q = (lambda x, *d: x.permute(*d))((x @ wq).reshape(B, S, H, Dh), 0, 2, 1, 3)
        k = x @ wk
        v = x @ wv
        scores = torch.einsum("bhqd,bkd->bhqk", q, k) * (Dh ** -0.5)
        p = _softmax(scores, axis=-1)
        o = torch.einsum("bhqk,bkd->bhqd", p, v)
        return (lambda x, *d: x.permute(*d))(o, 0, 2, 1, 3).reshape(B, S, H * Dh) @ wo

    x = A_x
    x = x + _A_ref(_layernorm(x, axis=-1), A_wqkv, A_wo)
    x = x + _B_ref(_layernorm(x, axis=-1), B_wq, B_wk, B_wv, B_wo)
    x = x + _A_ref(_layernorm(x, axis=-1), A_wqkv, A_wo)
    x = x + _B_ref(_layernorm(x, axis=-1), B_wq, B_wk, B_wv, B_wo)
    return x
```

```cpp
#include <hip/hip_runtime.h>
#include <hip/hip_bf16.h>
#include <stdint.h>

typedef __bf16 bf16x8 __attribute__((ext_vector_type(8)));
typedef __bf16 bf16x2 __attribute__((ext_vector_type(2)));
typedef float f32x2 __attribute__((ext_vector_type(2)));
typedef float f32x4 __attribute__((ext_vector_type(4)));
typedef float f32x16 __attribute__((ext_vector_type(16)));
typedef unsigned u32x2 __attribute__((ext_vector_type(2)));
typedef unsigned u32x4 __attribute__((ext_vector_type(4)));
typedef short s16x4 __attribute__((ext_vector_type(4)));
#define DI __device__ __forceinline__
#define LAS __attribute__((address_space(3)))
#define GAS __attribute__((address_space(1)))
typedef GAS unsigned gu32;
#define RLX_AGENT __ATOMIC_RELAXED, __HIP_MEMORY_SCOPE_AGENT

constexpr int DM = 576;
constexpr unsigned XB = 128u * 72u * 256u;
constexpr unsigned OAB = 128u * 80u * 256u;
constexpr unsigned KVT = 2432u;
constexpr unsigned KVB = 64u * KVT;
constexpr unsigned O_CNT = 0;
constexpr unsigned O_XP0 = 131072;
constexpr unsigned O_OA = O_XP0 + XB;
constexpr unsigned O_X1 = O_OA + 2 * OAB;
constexpr unsigned O_X2 = O_X1 + 2 * XB;
constexpr unsigned O_OB = O_X2 + XB;
constexpr unsigned O_KV = O_OB + 2 * XB;
constexpr unsigned KVS = KVB + 1024;
constexpr unsigned STB = 16u * 128u * 128u;
constexpr unsigned O_ST = O_KV + 2 * KVS;
constexpr unsigned O_WA1 = O_ST + 4 * STB;
constexpr unsigned WA1S = 18u * 7168u;
constexpr unsigned O_WOA = O_WA1 + 16 * WA1S;
constexpr unsigned WOAS = 20u * 4608u;
constexpr unsigned WQS = 18u * 4608u, WKS = 18u * 1152u;
constexpr unsigned O_WQ = O_WOA + 8 * WOAS;
constexpr unsigned KVWS = 43u * 1024u;
constexpr unsigned O_KVW = O_WQ + 8 * WQS;
constexpr unsigned O_WOB = O_KVW + 2 * KVWS;
constexpr unsigned O_CSP = O_WOB + 8 * WQS;
constexpr unsigned O_CSPA = O_CSP + 18 * 8 * 112 * 4;
constexpr unsigned O_CS = O_CSPA + 18 * 16 * 112 * 4;
constexpr unsigned WS_USED = O_CS + 256 * 224 * 4;
constexpr int C_OA = 0, C_X1 = 32, C_OB = 96, C_X2 = 160, C_KV = 256, KV_LINES = 64, C_TMO = 400;
constexpr float QSCALE = 0.23570226039551584f * 1.4426950408889634f;
constexpr float LN_EPS = 1e-5f;
constexpr float SAFE_BOUND2 = 3600.f;
constexpr float SAFE_VMAX = 1.125899906842624e15f;
constexpr unsigned SPIN_CAP = 1u << 17;

constexpr unsigned LDS_BYTES = 163840;
constexpr unsigned L_SCR = 129024;
constexpr unsigned L_SCR2 = 136192;
constexpr unsigned L_QS = 143360;
constexpr unsigned L_QKV = 94208;
constexpr unsigned L_CS = 146432;
constexpr unsigned L_S3 = 106496;
constexpr unsigned L_AE = 98304;

DI unsigned pk2(float lo, float hi) { f32x2 v = {lo, hi}; bf16x2 b = __builtin_convertvector(v, bf16x2); return __builtin_bit_cast(unsigned, b); }
DI float bflo(unsigned u) { return __uint_as_float(u << 16); }
DI float bfhi(unsigned u) { return __uint_as_float(u & 0xffff0000u); }
DI f32x4 mfma16(u32x4 a, u32x4 b, f32x4 c) { return __builtin_amdgcn_mfma_f32_16x16x32_bf16(__builtin_bit_cast(bf16x8, a), __builtin_bit_cast(bf16x8, b), c, 0, 0, 0); }
DI f32x16 mfma32(u32x4 a, u32x4 b, f32x16 c) { return __builtin_amdgcn_mfma_f32_32x32x16_bf16(__builtin_bit_cast(bf16x8, a), __builtin_bit_cast(bf16x8, b), c, 0, 0, 0); }
DI float dot2(unsigned a, unsigned b, float c) { return __builtin_amdgcn_fdot2_f32_bf16(__builtin_bit_cast(bf16x2, a), __builtin_bit_cast(bf16x2, b), c, false); }
DI float sumsq8(u32x4 v, float c) { unsigned a = v[0], b = v[1], d = v[2], e = v[3]; c = dot2(a, a, c); c = dot2(b, b, c); c = dot2(d, d, c); return dot2(e, e, c); }
DI float sum8(u32x4 v, float c) { unsigned a = v[0], b = v[1], d = v[2], e = v[3]; const unsigned o = 0x3f803f80u; c = dot2(a, o, c); c = dot2(b, o, c); c = dot2(d, o, c); return dot2(e, o, c); }
template <class F> DI float red16(float v, F op) { const unsigned u = __float_as_uint(v); const auto r = __builtin_amdgcn_permlane16_swap(u, u, false, false); return op(__uint_as_float(r[0]), __uint_as_float(r[1])); }
template <class F> DI float red32(float v, F op) { const unsigned u = __float_as_uint(v); const auto r = __builtin_amdgcn_permlane32_swap(u, u, false, false); return op(__uint_as_float(r[0]), __uint_as_float(r[1])); }
template <int N> DI float ror16(float v) { return __uint_as_float(__builtin_amdgcn_update_dpp(0u, __float_as_uint(v), 0x120 + N, 0xf, 0xf, false)); }
DI float max2(float a, float b) { return __builtin_amdgcn_fmed3f(a, b, __builtin_inff()); }
DI float max3(float a, float b, float c) { return max2(max2(a, b), c); }
struct OpAdd { DI float operator()(float a, float b) const { return a + b; } };
struct OpMax { DI float operator()(float a, float b) const { return max2(a, b); } };
template <class F> DI float red_g(float v, F op) { return red32(red16(v, op), op); }
template <class F> DI float red_t(float v, F op) { v = op(v, ror16<8>(v)); v = op(v, ror16<4>(v)); v = op(v, ror16<2>(v)); return op(v, ror16<1>(v)); }
DI float red_g(float v) { return red_g(v, OpAdd()); }
DI u32x4 lds16(const char LAS* p) { return *(const LAS u32x4*)p; }
DI f32x4 ldsf4(const char LAS* p) { return *(const LAS f32x4*)p; }
DI float ldsf(const char LAS* p) { return *(const LAS float*)p; }
DI void sts16(char LAS* p, u32x4 v) { *(LAS u32x4*)p = v; }
DI void stsf4(char LAS* p, f32x4 v) { *(LAS f32x4*)p = v; }
DI void sts4(char LAS* p, unsigned v) { *(LAS unsigned*)p = v; }
DI void stsf(char LAS* p, float v) { *(LAS float*)p = v; }
typedef short v4i16_t __attribute__((ext_vector_type(4)));
DI u32x2 ldstr(const char LAS* p) { return __builtin_bit_cast(u32x2, __builtin_amdgcn_ds_read_tr16_b64_v4i16((LAS v4i16_t*)p)); }
typedef __amdgpu_buffer_rsrc_t rsrc_t;
DI u32x4 gld16(rsrc_t r, unsigned voff, unsigned soff) { return __builtin_amdgcn_raw_buffer_load_b128(r, voff, soff, 16); }
DI u32x2 gld8(rsrc_t r, unsigned voff, unsigned soff) { return __builtin_amdgcn_raw_buffer_load_b64(r, voff, soff, 16); }
DI unsigned gld4(rsrc_t r, unsigned voff, unsigned soff) { return __builtin_amdgcn_raw_buffer_load_b32(r, voff, soff, 16); }
DI f32x4 gldc4(rsrc_t r, unsigned voff, unsigned soff) { return __builtin_bit_cast(f32x4, __builtin_amdgcn_raw_buffer_load_b128(r, voff, soff, 0)); }
DI float gldc1(rsrc_t r, unsigned voff, unsigned soff) { return __uint_as_float(__builtin_amdgcn_raw_buffer_load_b32(r, voff, soff, 0)); }
#define SBAR() __builtin_amdgcn_sched_barrier(0)
DI void gst16(rsrc_t r, u32x4 v, unsigned voff, unsigned soff) { __builtin_amdgcn_raw_buffer_store_b128(v, r, voff, soff, 16); }
DI void gst8(rsrc_t r, u32x2 v, unsigned voff, unsigned soff) { __builtin_amdgcn_raw_buffer_store_b64(v, r, voff, soff, 16); }
DI void gst4(rsrc_t r, unsigned v, unsigned voff, unsigned soff) { __builtin_amdgcn_raw_buffer_store_b32(v, r, voff, soff, 16); }
#define WG_BARRIER() asm volatile("s_waitcnt lgkmcnt(0)\n\ts_barrier" ::: "memory")
#define WAIT_VM(n) asm volatile("s_waitcnt vmcnt(" #n ")" ::: "memory")
#define WAIT_VMN(n) asm volatile("s_waitcnt vmcnt(%0)" :: "n"(n) : "memory")
#define GLDS(o) "global_load_lds_dwordx4 %1, %2 offset:" #o
#define DMA_ASM(body) asm volatile("s_mov_b32 %0, m0\n\ts_mov_b32 m0, %3\n\ts_nop 0\n\t" body "\n\ts_mov_b32 m0, %0" : "=&s"(sv) : "v"(voff), "s"(gbase), "s"(lds_addr) : "memory")
template <int N, bool SC1> DI void dma_upto4(const char* gbase, unsigned voff, unsigned lds_addr) {
    unsigned sv;
    if constexpr (!SC1) {
        if constexpr (N == 1) DMA_ASM(GLDS(0));
        if constexpr (N == 2) DMA_ASM(GLDS(0) "\n\t" GLDS(1024));
        if constexpr (N == 3) DMA_ASM(GLDS(0) "\n\t" GLDS(1024) "\n\t" GLDS(2048));
        if constexpr (N == 4) DMA_ASM(GLDS(0) "\n\t" GLDS(1024) "\n\t" GLDS(2048) "\n\t" GLDS(3072));
    } else {
        if constexpr (N == 1) DMA_ASM(GLDS(0) " sc1");
        if constexpr (N == 4) DMA_ASM(GLDS(0) " sc1\n\t" GLDS(1024) " sc1\n\t" GLDS(2048) " sc1\n\t" GLDS(3072) " sc1");
    }
}
template <int N, bool SC1> DI void dma_run(const char* gbase, unsigned voff, unsigned lds_addr) {
    if constexpr (N > 0) {
        dma_upto4<(N < 4 ? N : 4), SC1>(gbase, voff, lds_addr);
        dma_run<(N < 4 ? 0 : N - 4), SC1>(gbase, voff + 4096, lds_addr + 4096);
    }
}
template <int NP, int K0, int K1> DI void dma_image(const char* g, unsigned lds_addr, int w, unsigned lane16) {
    constexpr int K = (NP + 6) / 7;
    asm volatile("" : "+s"(w), "+s"(g), "+v"(lane16));
    if (w > 0) { const unsigned o = (min((w - 1) * K, NP - K) + K0) * 1024; dma_run<K1 - K0, false>(g, lane16 + o, lds_addr + o); }
}
DI gu32* cnt_ptr(char* ws, int idx) { return (gu32*)(ws + O_CNT) + idx * 64; }
DI void wait_counters(char* ws, int idx, int n, unsigned target, int lane) {
    asm volatile("" : "+s"(ws), "+s"(idx), "+v"(lane));
    gu32* p = cnt_ptr(ws, idx + (lane < n ? lane : 0));
    gu32* tmo = cnt_ptr(ws, C_TMO);
    for (unsigned spins = 0;;) {
        const unsigned v = __hip_atomic_load(p, RLX_AGENT);
        if (__all(v >= target)) break;
        ++spins;
        if (spins > SPIN_CAP) { if (lane == 0) __hip_atomic_store(tmo, 1u, RLX_AGENT); break; }
        if ((spins & 63u) == 0u && __builtin_amdgcn_readfirstlane(__hip_atomic_load(tmo, RLX_AGENT))) break;
    }
}
DI void signal_counter(char* ws, int idx, int rep) {
    asm volatile("" : "+s"(ws), "+s"(idx), "+v"(rep));
    __hip_atomic_fetch_add(cnt_ptr(ws, idx + rep * 32), 1u, RLX_AGENT);
}

DI int oa_row(int kp) { const int hp = kp / 40, s = kp % 40; return 36 * hp + (s < 16 ? s : (s < 32 ? s + 4 : (s < 36 ? s - 16 : 0))); }
DI int ob_row(int kp) { const int s = kp % 72; return kp - s + (s < 64 ? (s >> 4) * 18 + (s & 15) : ((s - 64) >> 1) * 18 + 16 + (s & 1)); }
DI u32x4 tr_frag(const char LAS* tile, int stride, int g, int t, int colbyte) {
    const char LAS* p = tile + (8 * g + (t >> 2)) * stride + colbyte;
    const u32x2 a = ldstr(p), b = ldstr(p + 4 * stride);
    return (u32x4){a[0], a[1], b[0], b[1]};
}
constexpr int P_A = 144, P_B = P_A + 224, P_C = P_B + 60, P_N = P_C + 128;
__global__ void __launch_bounds__(256) prep_kernel(const unsigned short* __restrict__ x, const unsigned short* __restrict__ wqkv, const unsigned short* __restrict__ woa,
                                                   const unsigned short* __restrict__ wq, const unsigned short* __restrict__ wk, const unsigned short* __restrict__ wv,
                                                   const unsigned short* __restrict__ wob, char* ws) {
    __shared__ __attribute__((aligned(16))) char smem[16384];
    char LAS* sm = (char LAS*)smem;
    const rsrc_t rs = __builtin_amdgcn_make_buffer_rsrc((void*)ws, 0, WS_USED, 0x00020000);
    const int tid = threadIdx.x, b = blockIdx.x, lane = tid & 63, w = __builtin_amdgcn_readfirstlane(tid >> 6), t = lane & 15, g = lane >> 4, p4 = t & 3;
    unsigned pf0 = 0, pf1 = 0, pf2 = 0;
    if (w == 3) {
        unsigned long long pc; unsigned len, lenm; int dm;
        asm volatile("s_getpc_b64 %0\nLprep_here:\n\ts_mov_b32 %1, Lprep_code_end-Lprep_here\n\ts_mov_b32 %2, _Z11main_kernelPcPt-Lprep_here\n\t"
                     "s_mov_b32 %3, Lmain_code_end-_Z11main_kernelPcPt" : "=s"(pc), "=&s"(len), "=&s"(dm), "=&s"(lenm));
        const unsigned long long pm = pc + (long long)dm;
        pf0 = *(const GAS unsigned*)(pc + min((unsigned)lane * 128, len - 4));
        pf1 = *(const GAS unsigned*)(pc + min((unsigned)(64 + lane) * 128, len - 4));
        if (lane < 4) pf2 = *(const GAS unsigned*)(pm + min((unsigned)((b >> 3) * 4 + lane) * 128, lenm - 4));
    }
    if (b < 256 && tid < 32) gst16(rs, (u32x4){0u, 0u, 0u, 0u}, (b * 32 + tid) * 16, O_CNT);
    if (b < P_A) {
        const int ks = b >> 3, hq = b & 7; constexpr int ST = 448;
        u32x4 v[4];
#pragma unroll
        for (int it = 0; it < 4; ++it) { const int c = min(tid + it * 256, 863), r = c / 27, q = c - 27 * r, pt = q / 9; v[it] = *(const u32x4*)(wqkv + (32 * ks + r) * 1728 + pt * 576 + 72 * hq + (q - 9 * pt) * 8); }
#pragma unroll
        for (int it = 0; it < 4; ++it) { const int c = min(tid + it * 256, 863), r = c / 27, q = c - 27 * r; sts16(sm + r * ST + q * 16, v[it]); }
        __syncthreads();
#pragma unroll
        for (int it = 0; it < 4; ++it) {
            const int f = min(w + it * 4, 13), hl = f >= 7, j = f - 7 * hl;
            const int cb = hl * 72 + (j < 6 ? (j >> 1) * 144 + (j & 1) * 40 + p4 * 8 : min(p4, 2) * 144 + 32);
            u32x4 fr = tr_frag(sm, ST, g, t, cb);
            if (j == 6 && t >= 12) fr = (u32x4){0u, 0u, 0u, 0u};
            gst16(rs, fr, lane * 16, O_WA1 + (2 * hq + hl) * WA1S + ks * 7168 + j * 1024);
            const float cs = red_g(sum8(fr, 0.f));
            if (g == 0) gst4(rs, __float_as_uint(cs), t * 4, O_CSPA + ((ks * 16 + 2 * hq + hl) * 112 + 16 * j) * 4);
        }
    } else if (b < P_B) {
        const int bb = b - P_A, blk = bb >> 2, mq = bb & 3;
        const int which = blk < 20 ? 0 : (blk < 38 ? 1 : 2), ks = blk - (which == 0 ? 0 : (which == 1 ? 20 : 38)); constexpr int ST = 304;
        const unsigned short* src = which == 0 ? woa : (which == 1 ? wq : wob);
        u32x4 v[3];
#pragma unroll
        for (int it = 0; it < 3; ++it) {
            const int c = min(tid + it * 256, 575), r = c / 18, q = c - 18 * r, kp = 32 * ks + r, k = which == 0 ? oa_row(kp) : (which == 2 ? ob_row(kp) : kp);
            v[it] = *(const u32x4*)(src + k * DM + 144 * mq + q * 8);
            if (which == 0 && kp % 40 >= 36) v[it] = (u32x4){0u, 0u, 0u, 0u};
        }
        const bool kvt = which == 1 && mq == 0;
        unsigned kvv[3];
        if (kvt) {
#pragma unroll
            for (int it = 0; it < 3; ++it) { const int c = min(tid + it * 256, 575), r = c / 18, q = c - 18 * r; kvv[it] = ((const unsigned*)(q < 9 ? wk : wv))[(32 * ks + r) * 9 + (q < 9 ? q : q - 9)]; }
        }
#pragma unroll
        for (int it = 0; it < 3; ++it) { const int c = min(tid + it * 256, 575), r = c / 18, q = c - 18 * r; sts16(sm + r * ST + q * 16, v[it]); }
        if (kvt) {
#pragma unroll
            for (int it = 0; it < 3; ++it) { const int c = min(tid + it * 256, 575), r = c / 18, q = c - 18 * r; sts4(sm + 32 * ST + r * 96 + (q < 9 ? q * 4 : 48 + (q - 9) * 4), kvv[it]); }
        }
        __syncthreads();
        const unsigned img = (which == 0 ? O_WOA : (which == 1 ? O_WQ : O_WOB)), ims = which == 0 ? WOAS : WQS;
#pragma unroll
        for (int it = 0; it < 3; ++it) {
            const int f = min(w + it * 4, 9), ml = f >= 5, j = f - 5 * ml, m = 2 * mq + ml;
            int cb;
            if (which == 1) cb = j < 4 ? (j >> 1) * 72 + (j & 1) * 40 + p4 * 8 : (p4 & 1) * 72 + 32; else cb = j * 32 + (j < 4 ? p4 : (p4 & 1)) * 8;
            const u32x4 fr = tr_frag(sm, ST, g, t, ml * 144 + cb);
            const unsigned dst = img + m * ims + ks * 4608;
            if (j < 4) gst16(rs, fr, lane * 16, dst + j * 1024); else if (t < 8) gst16(rs, fr, (g * 8 + t) * 16, dst + 4096);
            if (which == 1) { const float cs = red_g(sum8(fr, 0.f)); if (g == 0 && (j < 4 || t < 8)) gst4(rs, __float_as_uint(cs), t * 4, O_CSP + ((ks * 8 + m) * 112 + 16 * j) * 4); }
        }
        if (kvt) {
            const int isv = w >> 1, j = w & 1;
            const u32x4 fr = tr_frag(sm + 32 * ST, 96, g, t, isv * 48 + (j ? 32 : p4 * 8));
            const float cs = red_g(sum8(fr, 0.f));
            const unsigned dst = O_KVW + isv * KVWS + ks * 1152;
            if (j == 0) gst16(rs, fr, lane * 16, dst); else if (t < 2) gst16(rs, fr, (g * 2 + t) * 16, dst + 1024);
#pragma unroll
            for (int mm = 0; mm < 4; ++mm)
                if (g == 0 && (j == 0 || t < 2)) gst4(rs, __float_as_uint(cs), t * 4, O_CSP + ((ks * 8 + 2 * mm + isv) * 112 + 80 + 16 * j) * 4);
        }
    } else if (b < P_C) {
        const int bb = b - P_B, kp0 = bb / 3, nt = bb - 3 * kp0;
        const unsigned short* bsrc = nt == 0 ? wk + t : (nt == 1 ? wv + t : ((t & 2) ? wv : wk) + 16 + (t & 1));
        u32x4 af[2][5]; unsigned short e[5][8];
#pragma unroll
        for (int st = 0; st < 5; ++st) {
            const int n0 = 32 * min(w + 4 * st, 17) + 8 * g;
#pragma unroll
            for (int j = 0; j < 8; ++j) e[st][j] = bsrc[(n0 + j) * 18];
#pragma unroll
            for (int a = 0; a < 2; ++a) af[a][st] = *(const u32x4*)(woa + oa_row(32 * kp0 + 8 * (t >> 2) + (t & 3) + 4 * a) * DM + n0);
        }
        f32x4 d0 = {0.f, 0.f, 0.f, 0.f}, d1 = d0;
        const u32x4 z = {0u, 0u, 0u, 0u};
        const bool pad0 = (32 * kp0 + 8 * (t >> 2) + (t & 3)) % 40 >= 36, pad1 = (32 * kp0 + 8 * (t >> 2) + (t & 3) + 4) % 40 >= 36;
#pragma unroll
        for (int st = 0; st < 5; ++st) {
            if (w + 4 * st < 18) {
                const u32x4 bf = {e[st][0] | ((unsigned)e[st][1] << 16), e[st][2] | ((unsigned)e[st][3] << 16), e[st][4] | ((unsigned)e[st][5] << 16), e[st][6] | ((unsigned)e[st][7] << 16)};
                d0 = mfma16(pad0 ? z : af[0][st], bf, d0); d1 = mfma16(pad1 ? z : af[1][st], bf, d1);
            }
        }
        stsf4(sm + (w * 2) * 1024 + lane * 16, d0); stsf4(sm + (w * 2 + 1) * 1024 + lane * 16, d1);
        __syncthreads();
        if (w == 0) {
#pragma unroll
            for (int q = 1; q < 4; ++q) { d0 += ldsf4(sm + (q * 2) * 1024 + lane * 16); d1 += ldsf4(sm + (q * 2 + 1) * 1024 + lane * 16); }
            const u32x4 fr = {pk2(d0[0], d0[1]), pk2(d0[2], d0[3]), pk2(d1[0], d1[1]), pk2(d1[2], d1[3])};
            if (nt < 2) gst16(rs, fr, lane * 16, O_KVW + nt * KVWS + WKS + kp0 * 1152);
            else if (t < 4) gst16(rs, fr, 1024 + (g * 2 + (t & 1)) * 16, O_KVW + (t >> 1) * KVWS + WKS + kp0 * 1152);
        }
    } else {
        const int panel = b - P_C;
        u32x4 v[5];
#pragma unroll
        for (int it = 0; it < 5; ++it) { const int c = min(tid + it * 256, 1151), tt = c & 15, u = c >> 4; v[it] = *(const u32x4*)(x + (32 * (panel >> 1) + 2 * tt + (panel & 1)) * DM + u * 8); }
#pragma unroll
        for (int it = 0; it < 5; ++it) {
            const int c = tid + it * 256;
            if (c < 1152) { gst16(rs, v[it], c * 16, O_XP0 + panel * 1152 * 16); *(LAS f32x2*)(sm + c * 8) = (f32x2){sum8(v[it], 0.f), sumsq8(v[it], 0.f)}; }
        }
        __syncthreads();
        const int tt = tid & 15, p = tid >> 4; f32x2 a = {0.f, 0.f};
#pragma unroll
        for (int q = 0; q < 5; ++q) { const int u = p + 16 * q; if (u < 72) a += *(const LAS f32x2*)(sm + (u * 16 + tt) * 8); }
        gst8(rs, (u32x2){__float_as_uint(a[0]), __float_as_uint(a[1])}, p * 128 * 128 + tt * 8, O_ST + panel * 128);
    }
    asm volatile("Lprep_code_end:" :: "v"(pf0), "v"(pf1), "v"(pf2));
}

struct Ctx { char* ws; rsrc_t rs; char LAS* sm; int tid, lane, w, t, g; unsigned lane16, lds0; };

DI Ctx fresh(const Ctx& c0) {
    Ctx c = c0;
    int tid = c0.tid, w = c0.w; char* ws = c0.ws;
    asm volatile("" : "+v"(tid), "+s"(w), "+s"(ws));
    c.tid = tid; c.w = w; c.ws = ws; c.rs = __builtin_amdgcn_make_buffer_rsrc((void*)ws, 0, WS_USED, 0x00020000);
    c.lane = tid & 63; c.t = c.lane & 15; c.g = c.lane >> 4; c.lane16 = c.lane * 16;
    return c;
}
DI int opaque(int v) { asm volatile("" : "+s"(v)); return v; }

struct Resid { u32x2 v[3]; };

struct Stats { u32x2 p[4]; };
DI Stats stats_load(const Ctx& c, unsigned stbuf, int panel, int slot) {
    Stats st;
#pragma unroll
    for (int q = 0; q < 4; ++q) st.p[q] = gld8(c.rs, ((4 * c.g + q) * 128 + panel) * 128 + slot * 8, stbuf);
    return st;
}
DI void stats_finish(const Stats& st, float& mean, float& rstd) {
    float sm = 0.f, sq = 0.f;
#pragma unroll
    for (int q = 0; q < 4; ++q) { const unsigned a = st.p[q][0], b = st.p[q][1]; sm += __uint_as_float(a); sq += __uint_as_float(b); }
    mean = red_g(sm) * (1.f / DM);
    rstd = __builtin_amdgcn_rsqf(fmaxf(red_g(sq) * (1.f / DM) - mean * mean, 0.f) + LN_EPS);
}
template <int NKH> DI void proj_load(const Ctx& c, unsigned abase, u32x4 (&xf)[NKH]) {
#pragma unroll
    for (int s = 0; s < NKH; ++s) xf[s] = gld16(c.rs, c.lane16, abase + s * 1024);
}
template <int NKH, int KV> DI void proj_gemm(const Ctx& c, f32x4 (&acc)[5], const u32x4 (&xf)[NKH], unsigned kvimg, f32x4& kv0, f32x4& kv1) {
        constexpr int AH = KV ? 2 : 3;
    const int kh = c.w >> 2;
    const char LAS* wb = c.sm + kh * NKH * 4608;
    const char LAS* wA = wb + kh * 2048 + c.lane16;
    const char LAS* wB = wb + (1 - kh) * 2048 + c.lane16;
    const char LAS* w4 = wb + 4096 + (c.g * 8 + (c.t & 7)) * 16;
    const char LAS* wk = c.sm + kvimg + kh * NKH * 1152;
    const char LAS* wk0 = wk + c.lane16;
    const char LAS* wk1 = wk + 1024 + (c.g * 2 + (c.t & 1)) * 16;
    u32x4 wf[NKH][5], wg[NKH][2];
#define PW(s, j) lds16((j < 2 ? wA + j * 1024 : (j < 4 ? wB + (j - 2) * 1024 : w4)) + (s) * 4608)
#pragma unroll
    for (int s = 0; s < AH; ++s) {
        if (KV) { wg[s][0] = lds16(wk0 + s * 1152); wg[s][1] = lds16(wk1 + s * 1152); }
#pragma unroll
        for (int j = 0; j < 5; ++j) wf[s][j] = PW(s, j);
    }
#pragma unroll
    for (int j = 0; j < 5; ++j) acc[j] = (f32x4){0.f, 0.f, 0.f, 0.f};
    SBAR();
#pragma unroll
    for (int s = 0; s < NKH; ++s) {
        if (KV) {
            if (s + AH < NKH) { wg[s + AH][0] = lds16(wk0 + (s + AH) * 1152); wg[s + AH][1] = lds16(wk1 + (s + AH) * 1152); }
            kv0 = mfma16(wg[s][0], xf[s], kv0); kv1 = mfma16(wg[s][1], xf[s], kv1);
            SBAR();
        }
#pragma unroll
        for (int j = 0; j < 5; ++j) {
            if (s + AH < NKH) wf[s + AH][j] = PW(s + AH, j);
            acc[j] = mfma16(wf[s][j], xf[s], acc[j]);
            SBAR();
        }
    }
#undef PW
}
DI void kv_input_gemm(const Ctx& c, const u32x4 (&xi)[9], unsigned img, f32x4& kv0, f32x4& kv1) {
    const char LAS* wk = c.sm + img + (c.w >> 2) * 9 * 1152;
    const char LAS* p0 = wk + c.lane16;
    const char LAS* p1 = wk + 1024 + (c.g * 2 + (c.t & 1)) * 16;
    u32x4 a[9], b[9];
#pragma unroll
    for (int s = 0; s < 4; ++s) { a[s] = lds16(p0 + s * 1152); b[s] = lds16(p1 + s * 1152); }
    SBAR();
#pragma unroll
    for (int s = 0; s < 9; ++s) {
        if (s + 4 < 9) { a[s + 4] = lds16(p0 + (s + 4) * 1152); b[s + 4] = lds16(p1 + (s + 4) * 1152); }
        kv0 = mfma16(a[s], xi[s], kv0); kv1 = mfma16(b[s], xi[s], kv1);
        SBAR();
    }
}
template <int NKH> DI void proj_gemm(const Ctx& c, f32x4 (&acc)[5], const u32x4 (&xf)[NKH]) { f32x4 a, b; proj_gemm<NKH, 0>(c, acc, xf, 0, a, b); }
DI void proj_reduce(const Ctx& c, unsigned scr, const f32x4 (&acc)[5], f32x4 (&r)[3]) {
    char LAS* mine = c.sm + scr + c.w * 3072 + c.lane16;
    const char LAS* other = c.sm + scr + (c.w ^ 4) * 3072 + c.lane16;
    stsf4(mine, acc[2]); stsf4(mine + 1024, acc[3]); stsf4(mine + 2048, acc[4]);
    WG_BARRIER();
    r[0] = acc[0] + ldsf4(other); r[1] = acc[1] + ldsf4(other + 1024); r[2] = acc[4] + ldsf4(other + 2048);
}
DI void proj_finish(const Ctx& c, int grp, int m, const f32x4 (&r)[3], Resid& res, unsigned xout, unsigned stout, bool last, unsigned short* out) {
    const int i = c.w & 3, kh = c.w >> 2, panel = 4 * grp + i;
#pragma unroll
    for (int q = 0; q < 3; ++q) {
        const unsigned a = res.v[q][0], b = res.v[q][1];
        res.v[q] = (u32x2){pk2(bflo(a) + r[q][0], bfhi(a) + r[q][1]), pk2(bflo(b) + r[q][2], bfhi(b) + r[q][3])};
    }
    const auto s0 = __builtin_amdgcn_permlane16_swap(res.v[0][0], res.v[1][0], false, false);
    const auto s1 = __builtin_amdgcn_permlane16_swap(res.v[0][1], res.v[1][1], false, false);
    const u32x4 pa = {s0[0], s1[0], s0[1], s1[1]};
    const int ua = 9 * m + 4 * kh + 2 * (c.g & 1) + (c.g >> 1);
    const auto t0 = __builtin_amdgcn_permlane16_swap(res.v[2][0], res.v[2][0], false, false);
    const auto t1 = __builtin_amdgcn_permlane16_swap(res.v[2][1], res.v[2][1], false, false);
    const u32x4 pb = {t0[0], t1[0], t0[1], t1[1]};
    if (!last) {
        gst16(c.rs, pa, ua * 256 + c.t * 16, xout + panel * 72 * 256);
        const bool tail = kh == 0 && c.g == 0;
        if (tail) gst16(c.rs, pb, (9 * m + 8) * 256 + c.t * 16, xout + panel * 72 * 256);
        float sm = sum8(pa, 0.f), sq = sumsq8(pa, 0.f);
        if (tail) { sm = sum8(pb, sm); sq = sumsq8(pb, sq); }
        sm = red_g(sm); sq = red_g(sq);
        if (c.g == 0) gst8(c.rs, (u32x2){__float_as_uint(sm), __float_as_uint(sq)}, ((2 * m + kh) * 128 + panel) * 128 + c.t * 8, stout);
    } else {
        const rsrc_t ro = __builtin_amdgcn_make_buffer_rsrc((void*)out, 0, 2048 * DM * 2, 0x00020000);
        const int row = 32 * (panel >> 1) + 2 * c.t + (panel & 1);
        gst16(ro, pa, (row * DM + ua * 8) * 2, 0);
        if (kh == 0 && c.g == 0) gst16(ro, pb, (row * DM + (9 * m + 8) * 8) * 2, 0);
    }
}
DI void load_resid(const Ctx& c, int grp, int m, unsigned xin, Resid& res) {
    const int i = c.w & 3, kh = c.w >> 2;
    const unsigned base = xin + (4 * grp + i) * 72 * 256;
    const unsigned lo = (9 * m + (c.g >> 1)) * 256 + c.t * 16 + (c.g & 1) * 8;
    res.v[0] = gld8(c.rs, lo + (kh ? 4 : 0) * 256, base);
    res.v[1] = gld8(c.rs, lo + (kh ? 6 : 2) * 256, base);
    res.v[2] = gld8(c.rs, (9 * m + 8) * 256 + c.t * 16 + (c.g & 1) * 8, base);
}

DI void stage1(const Ctx& c, int layer, int R, int hp, unsigned xin, unsigned stin, unsigned oa) {
    (void)layer;
    const int i = c.w & 3, kh = c.w >> 2, t = c.t, g = c.g;
    u32x4 xa[9], xb[9], xh[9];
    const unsigned pa = xin + ((8 * R + 2 * i + kh) * 72 + 36 * kh) * 256, pb_ = xin + ((8 * R + 2 * i + 1 - kh) * 72 + 36 * kh) * 256;
    const int hpanel = (R > 0 ? 8 * R - 2 : 0) + (t >> 3);
    const unsigned hoff = (hpanel * 72 + g) * 256 + (8 + (t & 7)) * 16;
    constexpr int AHEAD = 2;
#define S1_LOAD(s) do { xa[s] = gld16(c.rs, c.lane16, pa + (s) * 1024); xb[s] = gld16(c.rs, c.lane16, pb_ + (s) * 1024); \
                        xh[s] = gld16(c.rs, hoff, xin + (36 * kh) * 256 + (s) * 1024); } while (0)
#pragma unroll
    for (int s = 0; s < AHEAD; ++s) S1_LOAD(s);
    SBAR();
    const char LAS* wp = c.sm + kh * 9 * 7168 + c.lane16;
    f32x4 aa[7], ab[7], ah0 = {0.f, 0.f, 0.f, 0.f}, ah1 = ah0;
    u32x4 wf[2][7];
#pragma unroll
    for (int j = 0; j < 7; ++j) { aa[j] = ah0; ab[j] = ah0; wf[0][j] = lds16(wp + j * 1024); }
    Stats sto, sth; float csv = 0.f;
    SBAR();
#pragma unroll
    for (int s = 0; s < 9; ++s) {
        if (s + AHEAD < 9) { S1_LOAD(s + AHEAD); SBAR(); }
#pragma unroll
        for (int j = 0; j < 7; ++j) {
            if (s + 1 < 9) wf[(s + 1) & 1][j] = lds16(wp + (s + 1) * 7168 + j * 1024);
            aa[j] = mfma16(wf[s & 1][j], xa[s], aa[j]); ab[j] = mfma16(wf[s & 1][j], xb[s], ab[j]);
            SBAR();
        }
        if (i == 0) { ah0 = mfma16(wf[s & 1][0], xh[s], ah0); ah1 = mfma16(wf[s & 1][1], xh[s], ah1); }
        else if (i == 1) { ah0 = mfma16(wf[s & 1][2], xh[s], ah0); ah1 = mfma16(wf[s & 1][3], xh[s], ah1); }
        else if (i == 2) { ah0 = mfma16(wf[s & 1][4], xh[s], ah0); ah1 = mfma16(wf[s & 1][5], xh[s], ah1); }
        else ah0 = mfma16(wf[s & 1][6], xh[s], ah0);
        SBAR();
        if (s == 6) {
            sto = stats_load(c, stin, 8 * R + 2 * i + kh, t); sth = stats_load(c, stin, hpanel, 8 + (t & 7));
            csv = gldc1(c.rs, (blockIdx.x * 224 + 112 + (c.tid < 112 ? c.tid : 0)) * 4, O_CS);
            SBAR();
        }
    }
#undef S1_LOAD
    float mean, rstd, hmean, hrstd;
    stats_finish(sto, mean, rstd); stats_finish(sth, hmean, hrstd);
    if (c.tid < 112) stsf(c.sm + L_CS + c.tid * 4, csv);
    WG_BARRIER();
    constexpr unsigned L_HP = 57344; constexpr int RS = 304;
    {
        char LAS* y = c.sm + c.w * 7168 + c.lane16;
#pragma unroll
        for (int j = 0; j < 7; ++j) stsf4(y + j * 1024, ab[j]);
        char LAS* hp_ = c.sm + L_HP + (kh * 7 + 2 * i) * 1024 + c.lane16;
        stsf4(hp_, ah0);
        if (i < 3) stsf4(hp_ + 1024, ah1);
        if (c.tid < 192) { const int rw = (c.tid * 43691) >> 19; sts16(c.sm + L_QKV + ((rw >> 3) * 16 + (rw & 7)) * RS + 96 + (c.tid - 12 * rw) * 16, (u32x4){0u, 0u, 0u, 0u}); }
    }
    WG_BARRIER();
    {
        const char LAS* y = c.sm + (c.w ^ 4) * 7168 + c.lane16;
        const int wh = c.w < 7 ? c.w : 6;
        f32x4 v[7], cs[7];
#pragma unroll
        for (int j = 0; j < 7; ++j) { v[j] = ldsf4(y + j * 1024); cs[j] = ldsf4(c.sm + L_CS + (16 * j + 4 * g) * 4); }
        const f32x4 vh = ldsf4(c.sm + L_HP + wh * 1024 + c.lane16) + ldsf4(c.sm + L_HP + (7 + wh) * 1024 + c.lane16), csh = ldsf4(c.sm + L_CS + (16 * wh + 4 * g) * 4);
        char LAS* row = c.sm + L_QKV + ((2 + 2 * i + kh) * 16 + t) * RS;
        char LAS* rowh = c.sm + L_QKV + ((t >> 3) * 16 + 8 + (t & 7)) * RS;
        const int offL = g < 3 ? g * 96 + 32 : 288, dL = g < 3 ? 48 : 4;
        const float rq = rstd * QSCALE, r6 = g == 0 ? rq : rstd;
#pragma unroll
        for (int j = 0; j < 7; ++j) {
            const f32x4 a = (aa[j] + v[j] - mean * cs[j]) * (j < 2 ? rq : (j < 6 ? rstd : r6));
            if (j < 6) *(LAS u32x2*)(row + j * 48 + g * 8) = (u32x2){pk2(a[0], a[1]), pk2(a[2], a[3])};
            else { sts4(row + offL, pk2(a[0], a[1])); sts4(row + offL + dL, pk2(a[2], a[3])); }
        }
        const f32x4 a = (vh - hmean * csh) * hrstd;
        char LAS* d = rowh + (wh == 6 ? offL : wh * 48 + g * 8);
        sts4(d, pk2(a[0], a[1])); sts4(d + (wh == 6 ? dL : 4), pk2(a[2], a[3]));
    }
    WG_BARRIER();
    {
        const char LAS* rp = c.sm + L_QKV + (c.w * 16) * RS;
        const char LAS* rc = rp + 32 * RS;
        const unsigned m0 = g < 3 ? 0xffffffffu : 0u, m1 = g < 2 ? 0xffffffffu : 0u;
        const u32x4 km = {m0, m1, m1, m1};
        const f32x4 z = {0.f, 0.f, 0.f, 0.f};
        u32x4 qf[2], kp[2], kc[2]; u32x2 va[2][2], vb[2][2];
#pragma unroll
        for (int hd = 0; hd < 2; ++hd) {
            qf[hd] = lds16(rc + t * RS + hd * 48 + g * 16);
            kp[hd] = lds16(rp + t * RS + (2 + hd) * 48 + g * 16); kc[hd] = lds16(rc + t * RS + (2 + hd) * 48 + g * 16);
        }
#pragma unroll
        for (int hd = 0; hd < 2; ++hd)
#pragma unroll
            for (int dt = 0; dt < 2; ++dt) {
                const unsigned vo = (4 * g + (t >> 2)) * RS + (4 + hd) * 48 + dt * 32 + (t & 3) * 8;
                va[hd][dt] = ldstr(rp + vo); vb[hd][dt] = ldstr(rc + vo);
            }
        const int plo = (R == 0 && c.w < 2) ? 99 : t + 9;
        f32x4 bp, bc;
#pragma unroll
        for (int r = 0; r < 4; ++r) { const int tk = 4 * g + r; bp[r] = tk >= plo ? 0.f : -1.0e30f; bc[r] = (tk <= t && tk + 7 >= t) ? 0.f : -1.0e30f; }
        SBAR();
        f32x4 sp[2], sc[2];
#pragma unroll
        for (int hd = 0; hd < 2; ++hd) { const u32x4 q = qf[hd] & km; sp[hd] = mfma16(kp[hd] & km, q, bp); sc[hd] = mfma16(kc[hd] & km, q, bc); }
        u32x2 od[2]; unsigned ol[2];
#pragma unroll
        for (int hd = 0; hd < 2; ++hd) {
            const float mx = red_g(max3(max3(sp[hd][0], sp[hd][1], sp[hd][2]), max3(sp[hd][3], sc[hd][0], sc[hd][1]), max2(sc[hd][2], sc[hd][3])), OpMax());
            float pp[4], pc[4];
#pragma unroll
            for (int r = 0; r < 4; ++r) { pp[r] = __builtin_amdgcn_exp2f(sp[hd][r] - mx); pc[r] = __builtin_amdgcn_exp2f(sc[hd][r] - mx); }
            const u32x4 pf = {pk2(pp[0], pp[1]), pk2(pp[2], pp[3]), pk2(pc[0], pc[1]), pk2(pc[2], pc[3])};
            const f32x4 o0 = mfma16((u32x4){va[hd][0][0], va[hd][0][1], vb[hd][0][0], vb[hd][0][1]}, pf, z);
            const f32x4 o1 = mfma16((u32x4){va[hd][1][0], va[hd][1][1], vb[hd][1][0], vb[hd][1][1]}, pf, z);
            const float inv = __builtin_amdgcn_rcpf(red_g(((pp[0] + pp[1]) + (pp[2] + pp[3])) + ((pc[0] + pc[1]) + (pc[2] + pc[3]))));
            od[hd] = (u32x2){pk2(o0[0] * inv, o0[1] * inv), pk2(o0[2] * inv, o0[3] * inv)};
            ol[hd] = pk2(o1[0] * inv, o1[1] * inv);
        }
        const auto s0 = __builtin_amdgcn_permlane16_swap(od[0][0], od[1][0], false, false);
        const auto s1 = __builtin_amdgcn_permlane16_swap(od[0][1], od[1][1], false, false);
        const unsigned pb = oa + ((8 * R + c.w) * 80 + 5 * hp) * 256;
        gst16(c.rs, (u32x4){s0[0], s1[0], s0[1], s1[1]}, (2 * (g & 1) + (g >> 1)) * 256 + t * 16, pb);
        if (g == 0) gst16(c.rs, (u32x4){ol[0], ol[1], 0u, 0u}, 4 * 256 + t * 16, pb);
    }
}

#define ATTN_ASM \
    "s_mov_b32 %[m0s], m0\n\t" \
    "v_mov_b32 v168, 0\n\t" \
    "v_mov_b32 v169, 0\n\t" \
    "v_mov_b32 v170, 0\n\t" \
    "v_mov_b32 v171, 0\n\t" \
    "v_mov_b32 v172, 0\n\t" \
    "v_mov_b32 v173, 0\n\t" \
    "v_mov_b32 v174, 0\n\t" \
    "v_mov_b32 v175, 0\n\t" \
    "v_mov_b32 v184, 0\n\t" \
    "v_mov_b32 v185, 0\n\t" \
    "v_mov_b32 v186, 0\n\t" \
    "v_mov_b32 v187, 0\n\t" \
    "v_mov_b32 v188, 0\n\t" \
    "v_mov_b32 v189, 0\n\t" \
    "v_mov_b32 v190, 0\n\t" \
    "v_mov_b32 v191, 0\n\t" \
    "v_mov_b32 v181, %[kones]\n\t" \
    "v_mov_b32 v182, 0\n\t" \
    "v_mov_b32 v183, 0\n\t" \
    "ds_read_b128 v[176:179], %[ak0]\n\t" \
    "ds_read_b32 v180, %[ak1]\n\t" \
    "s_mov_b32 %[rd], 0\n\t" \
    "s_waitcnt lgkmcnt(0)\n\t" \
    "v_mfma_f32_32x32x16_bf16 v[128:143], v[176:179], %[qa0], 0\n\t" \
    "v_mfma_f32_32x32x16_bf16 v[128:143], v[180:183], %[qa1], v[128:143]\n\t" \
    "s_nop 7\n\t" \
    "s_nop 7\n\t" \
    "LATTN_LOOP_%=:\n\t" \
    "v_exp_f32 v128, v128\n\t" \
    "v_exp_f32 v129, v129\n\t" \
    "v_mfma_f32_32x32x16_bf16 v[144:159], v[176:179], %[qb0], 0\n\t" \
    "v_exp_f32 v130, v130\n\t" \
    "v_exp_f32 v131, v131\n\t" \
    "v_exp_f32 v132, v132\n\t" \
    "v_exp_f32 v133, v133\n\t" \
    "v_exp_f32 v134, v134\n\t" \
    "v_exp_f32 v135, v135\n\t" \
    "v_mfma_f32_32x32x16_bf16 v[144:159], v[180:183], %[qb1], v[144:159]\n\t" \
    "v_cvt_pk_bf16_f32 v160, v128, v129\n\t" \
    "ds_read_b128 v[176:179], %[ak0] offset:2432\n\t" \
    "ds_read_b32 v180, %[ak1] offset:2432\n\t" \
    "v_cvt_pk_bf16_f32 v161, v130, v131\n\t" \
    "v_exp_f32 v136, v136\n\t" \
    "v_exp_f32 v137, v137\n\t" \
    "v_exp_f32 v138, v138\n\t" \
    "v_exp_f32 v139, v139\n\t" \
    "v_mfma_f32_32x32x16_bf16 %[ob], v[184:187], v[168:171], %[ob]\n\t" \
    "v_cvt_pk_bf16_f32 v162, v132, v133\n\t" \
    "v_cvt_pk_bf16_f32 v163, v134, v135\n\t" \
    "v_exp_f32 v140, v140\n\t" \
    "v_exp_f32 v141, v141\n\t" \
    "v_exp_f32 v142, v142\n\t" \
    "v_exp_f32 v143, v143\n\t" \
    "v_mfma_f32_32x32x16_bf16 %[ob], v[188:191], v[172:175], %[ob]\n\t" \
    "v_cvt_pk_bf16_f32 v164, v136, v137\n\t" \
    "ds_read_b128 v[184:187], %[av] offset:0\n\t" \
    "ds_read_b128 v[188:191], %[av] offset:640\n\t" \
    "v_cvt_pk_bf16_f32 v165, v138, v139\n\t" \
    "v_cvt_pk_bf16_f32 v166, v140, v141\n\t" \
    "v_cvt_pk_bf16_f32 v167, v142, v143\n\t" \
    "v_exp_f32 v144, v144\n\t" \
    "v_exp_f32 v145, v145\n\t" \
    "s_waitcnt lgkmcnt(2)\n\t" \
    "v_mfma_f32_32x32x16_bf16 v[128:143], v[176:179], %[qa0], 0\n\t" \
    "v_exp_f32 v146, v146\n\t" \
    "v_exp_f32 v147, v147\n\t" \
    "v_exp_f32 v148, v148\n\t" \
    "v_exp_f32 v149, v149\n\t" \
    "v_exp_f32 v150, v150\n\t" \
    "v_exp_f32 v151, v151\n\t" \
    "v_mfma_f32_32x32x16_bf16 v[128:143], v[180:183], %[qa1], v[128:143]\n\t" \
    "v_cvt_pk_bf16_f32 v168, v144, v145\n\t" \
    "v_cvt_pk_bf16_f32 v169, v146, v147\n\t" \
    "v_exp_f32 v152, v152\n\t" \
    "v_exp_f32 v153, v153\n\t" \
    "v_exp_f32 v154, v154\n\t" \
    "v_exp_f32 v155, v155\n\t" \
    "s_waitcnt lgkmcnt(0)\n\t" \
    "v_mfma_f32_32x32x16_bf16 %[oa], v[184:187], v[160:163], %[oa]\n\t" \
    "v_cvt_pk_bf16_f32 v170, v148, v149\n\t" \
    "v_cvt_pk_bf16_f32 v171, v150, v151\n\t" \
    "v_exp_f32 v156, v156\n\t" \
    "v_exp_f32 v157, v157\n\t" \
    "v_exp_f32 v158, v158\n\t" \
    "v_exp_f32 v159, v159\n\t" \
    "v_mfma_f32_32x32x16_bf16 %[oa], v[188:191], v[164:167], %[oa]\n\t" \
    "v_cvt_pk_bf16_f32 v172, v152, v153\n\t" \
    "v_cvt_pk_bf16_f32 v173, v154, v155\n\t" \
    "s_cmp_lt_u32 %[rd], 3\n\t" \
    "s_cbranch_scc0 LD0_%=\n\t" \
    "s_mov_b32 m0, %[dl]\n\t" \
    "s_nop 0\n\t" \
    "global_load_lds_dwordx4 %[dv], %[kvg] offset:0 sc1\n\t" \
    "global_load_lds_dwordx4 %[dv], %[kvg] offset:1024 sc1\n\t" \
    "LD0_%=:\n\t" \
    "v_cvt_pk_bf16_f32 v174, v156, v157\n\t" \
    "v_cvt_pk_bf16_f32 v175, v158, v159\n\t" \
    "v_exp_f32 v128, v128\n\t" \
    "v_exp_f32 v129, v129\n\t" \
    "v_mfma_f32_32x32x16_bf16 v[144:159], v[176:179], %[qb0], 0\n\t" \
    "v_exp_f32 v130, v130\n\t" \
    "v_exp_f32 v131, v131\n\t" \
    "v_exp_f32 v132, v132\n\t" \
    "v_exp_f32 v133, v133\n\t" \
    "v_exp_f32 v134, v134\n\t" \
    "v_exp_f32 v135, v135\n\t" \
    "v_mfma_f32_32x32x16_bf16 v[144:159], v[180:183], %[qb1], v[144:159]\n\t" \
    "v_cvt_pk_bf16_f32 v160, v128, v129\n\t" \
    "ds_read_b128 v[176:179], %[ak0] offset:4864\n\t" \
    "ds_read_b32 v180, %[ak1] offset:4864\n\t" \
    "v_cvt_pk_bf16_f32 v161, v130, v131\n\t" \
    "v_exp_f32 v136, v136\n\t" \
    "v_exp_f32 v137, v137\n\t" \
    "v_exp_f32 v138, v138\n\t" \
    "v_exp_f32 v139, v139\n\t" \
    "v_mfma_f32_32x32x16_bf16 %[ob], v[184:187], v[168:171], %[ob]\n\t" \
    "v_cvt_pk_bf16_f32 v162, v132, v133\n\t" \
    "v_cvt_pk_bf16_f32 v163, v134, v135\n\t" \
    "v_exp_f32 v140, v140\n\t" \
    "v_exp_f32 v141, v141\n\t" \
    "v_exp_f32 v142, v142\n\t" \
    "v_exp_f32 v143, v143\n\t" \
    "v_mfma_f32_32x32x16_bf16 %[ob], v[188:191], v[172:175], %[ob]\n\t" \
    "v_cvt_pk_bf16_f32 v164, v136, v137\n\t" \
    "ds_read_b128 v[184:187], %[av] offset:2432\n\t" \
    "ds_read_b128 v[188:191], %[av] offset:3072\n\t" \
    "v_cvt_pk_bf16_f32 v165, v138, v139\n\t" \
    "v_cvt_pk_bf16_f32 v166, v140, v141\n\t" \
    "v_cvt_pk_bf16_f32 v167, v142, v143\n\t" \
    "v_exp_f32 v144, v144\n\t" \
    "v_exp_f32 v145, v145\n\t" \
    "s_waitcnt lgkmcnt(2)\n\t" \
    "v_mfma_f32_32x32x16_bf16 v[128:143], v[176:179], %[qa0], 0\n\t" \
    "v_exp_f32 v146, v146\n\t" \
    "v_exp_f32 v147, v147\n\t" \
    "v_exp_f32 v148, v148\n\t" \
    "v_exp_f32 v149, v149\n\t" \
    "v_exp_f32 v150, v150\n\t" \
    "v_exp_f32 v151, v151\n\t" \
    "v_mfma_f32_32x32x16_bf16 v[128:143], v[180:183], %[qa1], v[128:143]\n\t" \
    "v_cvt_pk_bf16_f32 v168, v144, v145\n\t" \
    "v_cvt_pk_bf16_f32 v169, v146, v147\n\t" \
    "v_exp_f32 v152, v152\n\t" \
    "v_exp_f32 v153, v153\n\t" \
    "v_exp_f32 v154, v154\n\t" \
    "v_exp_f32 v155, v155\n\t" \
    "s_waitcnt lgkmcnt(0)\n\t" \
    "v_mfma_f32_32x32x16_bf16 %[oa], v[184:187], v[160:163], %[oa]\n\t" \
    "v_cvt_pk_bf16_f32 v170, v148, v149\n\t" \
    "v_cvt_pk_bf16_f32 v171, v150, v151\n\t" \
    "v_exp_f32 v156, v156\n\t" \
    "v_exp_f32 v157, v157\n\t" \
    "v_exp_f32 v158, v158\n\t" \
    "v_exp_f32 v159, v159\n\t" \
    "v_mfma_f32_32x32x16_bf16 %[oa], v[188:191], v[164:167], %[oa]\n\t" \
    "v_cvt_pk_bf16_f32 v172, v152, v153\n\t" \
    "v_cvt_pk_bf16_f32 v173, v154, v155\n\t" \
    "s_cmp_lt_u32 %[rd], 3\n\t" \
    "s_cbranch_scc0 LD1_%=\n\t" \
    "global_load_lds_dwordx4 %[dv], %[kvg] offset:2048 sc1\n\t" \
    "LD1_%=:\n\t" \
    "v_cvt_pk_bf16_f32 v174, v156, v157\n\t" \
    "v_cvt_pk_bf16_f32 v175, v158, v159\n\t" \
    "v_exp_f32 v128, v128\n\t" \
    "v_exp_f32 v129, v129\n\t" \
    "v_mfma_f32_32x32x16_bf16 v[144:159], v[176:179], %[qb0], 0\n\t" \
    "v_exp_f32 v130, v130\n\t" \
    "v_exp_f32 v131, v131\n\t" \
    "v_exp_f32 v132, v132\n\t" \
    "v_exp_f32 v133, v133\n\t" \
    "v_exp_f32 v134, v134\n\t" \
    "v_exp_f32 v135, v135\n\t" \
    "v_mfma_f32_32x32x16_bf16 v[144:159], v[180:183], %[qb1], v[144:159]\n\t" \
    "v_cvt_pk_bf16_f32 v160, v128, v129\n\t" \
    "ds_read_b128 v[176:179], %[ak0] offset:7296\n\t" \
    "ds_read_b32 v180, %[ak1] offset:7296\n\t" \
    "v_cvt_pk_bf16_f32 v161, v130, v131\n\t" \
    "v_exp_f32 v136, v136\n\t" \
    "v_exp_f32 v137, v137\n\t" \
    "v_exp_f32 v138, v138\n\t" \
    "v_exp_f32 v139, v139\n\t" \
    "v_mfma_f32_32x32x16_bf16 %[ob], v[184:187], v[168:171], %[ob]\n\t" \
    "v_cvt_pk_bf16_f32 v162, v132, v133\n\t" \
    "v_cvt_pk_bf16_f32 v163, v134, v135\n\t" \
    "v_exp_f32 v140, v140\n\t" \
    "v_exp_f32 v141, v141\n\t" \
    "v_exp_f32 v142, v142\n\t" \
    "v_exp_f32 v143, v143\n\t" \
    "v_mfma_f32_32x32x16_bf16 %[ob], v[188:191], v[172:175], %[ob]\n\t" \
    "v_cvt_pk_bf16_f32 v164, v136, v137\n\t" \
    "ds_read_b128 v[184:187], %[av] offset:4864\n\t" \
    "ds_read_b128 v[188:191], %[av] offset:5504\n\t" \
    "v_cvt_pk_bf16_f32 v165, v138, v139\n\t" \
    "v_cvt_pk_bf16_f32 v166, v140, v141\n\t" \
    "v_cvt_pk_bf16_f32 v167, v142, v143\n\t" \
    "v_exp_f32 v144, v144\n\t" \
    "v_exp_f32 v145, v145\n\t" \
    "s_waitcnt lgkmcnt(2)\n\t" \
    "v_mfma_f32_32x32x16_bf16 v[128:143], v[176:179], %[qa0], 0\n\t" \
    "v_exp_f32 v146, v146\n\t" \
    "v_exp_f32 v147, v147\n\t" \
    "v_exp_f32 v148, v148\n\t" \
    "v_exp_f32 v149, v149\n\t" \
    "v_exp_f32 v150, v150\n\t" \
    "v_exp_f32 v151, v151\n\t" \
    "v_mfma_f32_32x32x16_bf16 v[128:143], v[180:183], %[qa1], v[128:143]\n\t" \
    "v_cvt_pk_bf16_f32 v168, v144, v145\n\t" \
    "v_cvt_pk_bf16_f32 v169, v146, v147\n\t" \
    "v_exp_f32 v152, v152\n\t" \
    "v_exp_f32 v153, v153\n\t" \
    "v_exp_f32 v154, v154\n\t" \
    "v_exp_f32 v155, v155\n\t" \
    "s_waitcnt lgkmcnt(0)\n\t" \
    "v_mfma_f32_32x32x16_bf16 %[oa], v[184:187], v[160:163], %[oa]\n\t" \
    "v_cvt_pk_bf16_f32 v170, v148, v149\n\t" \
    "v_cvt_pk_bf16_f32 v171, v150, v151\n\t" \
    "v_exp_f32 v156, v156\n\t" \
    "v_exp_f32 v157, v157\n\t" \
    "v_exp_f32 v158, v158\n\t" \
    "v_exp_f32 v159, v159\n\t" \
    "v_mfma_f32_32x32x16_bf16 %[oa], v[188:191], v[164:167], %[oa]\n\t" \
    "v_cvt_pk_bf16_f32 v172, v152, v153\n\t" \
    "v_cvt_pk_bf16_f32 v173, v154, v155\n\t" \
    "s_cmp_lt_u32 %[rd], 3\n\t" \
    "s_cbranch_scc0 LD2_%=\n\t" \
    "global_load_lds_dwordx4 %[dv], %[kvg] offset:3072 sc1\n\t" \
    "LD2_%=:\n\t" \
    "v_cvt_pk_bf16_f32 v174, v156, v157\n\t" \
    "v_cvt_pk_bf16_f32 v175, v158, v159\n\t" \
    "v_exp_f32 v128, v128\n\t" \
    "v_exp_f32 v129, v129\n\t" \
    "v_mfma_f32_32x32x16_bf16 v[144:159], v[176:179], %[qb0], 0\n\t" \
    "v_exp_f32 v130, v130\n\t" \
    "v_exp_f32 v131, v131\n\t" \
    "v_exp_f32 v132, v132\n\t" \
    "v_exp_f32 v133, v133\n\t" \
    "v_exp_f32 v134, v134\n\t" \
    "v_exp_f32 v135, v135\n\t" \
    "v_mfma_f32_32x32x16_bf16 v[144:159], v[180:183], %[qb1], v[144:159]\n\t" \
    "v_cvt_pk_bf16_f32 v160, v128, v129\n\t" \
    "ds_read_b128 v[176:179], %[ak0] offset:9728\n\t" \
    "ds_read_b32 v180, %[ak1] offset:9728\n\t" \
    "v_cvt_pk_bf16_f32 v161, v130, v131\n\t" \
    "v_exp_f32 v136, v136\n\t" \
    "v_exp_f32 v137, v137\n\t" \
    "v_exp_f32 v138, v138\n\t" \
    "v_exp_f32 v139, v139\n\t" \
    "v_mfma_f32_32x32x16_bf16 %[ob], v[184:187], v[168:171], %[ob]\n\t" \
    "v_cvt_pk_bf16_f32 v162, v132, v133\n\t" \
    "v_cvt_pk_bf16_f32 v163, v134, v135\n\t" \
    "v_exp_f32 v140, v140\n\t" \
    "v_exp_f32 v141, v141\n\t" \
    "v_exp_f32 v142, v142\n\t" \
    "v_exp_f32 v143, v143\n\t" \
    "v_mfma_f32_32x32x16_bf16 %[ob], v[188:191], v[172:175], %[ob]\n\t" \
    "v_cvt_pk_bf16_f32 v164, v136, v137\n\t" \
    "ds_read_b128 v[184:187], %[av] offset:7296\n\t" \
    "ds_read_b128 v[188:191], %[av] offset:7936\n\t" \
    "v_cvt_pk_bf16_f32 v165, v138, v139\n\t" \
    "v_cvt_pk_bf16_f32 v166, v140, v141\n\t" \
    "v_cvt_pk_bf16_f32 v167, v142, v143\n\t" \
    "v_exp_f32 v144, v144\n\t" \
    "v_exp_f32 v145, v145\n\t" \
    "s_waitcnt lgkmcnt(2)\n\t" \
    "v_mfma_f32_32x32x16_bf16 v[128:143], v[176:179], %[qa0], 0\n\t" \
    "v_exp_f32 v146, v146\n\t" \
    "v_exp_f32 v147, v147\n\t" \
    "v_exp_f32 v148, v148\n\t" \
    "v_exp_f32 v149, v149\n\t" \
    "v_exp_f32 v150, v150\n\t" \
    "v_exp_f32 v151, v151\n\t" \
    "v_mfma_f32_32x32x16_bf16 v[128:143], v[180:183], %[qa1], v[128:143]\n\t" \
    "v_cvt_pk_bf16_f32 v168, v144, v145\n\t" \
    "v_cvt_pk_bf16_f32 v169, v146, v147\n\t" \
    "v_exp_f32 v152, v152\n\t" \
    "v_exp_f32 v153, v153\n\t" \
    "v_exp_f32 v154, v154\n\t" \
    "v_exp_f32 v155, v155\n\t" \
    "s_waitcnt lgkmcnt(0)\n\t" \
    "v_mfma_f32_32x32x16_bf16 %[oa], v[184:187], v[160:163], %[oa]\n\t" \
    "v_cvt_pk_bf16_f32 v170, v148, v149\n\t" \
    "v_cvt_pk_bf16_f32 v171, v150, v151\n\t" \
    "v_exp_f32 v156, v156\n\t" \
    "v_exp_f32 v157, v157\n\t" \
    "v_exp_f32 v158, v158\n\t" \
    "v_exp_f32 v159, v159\n\t" \
    "v_mfma_f32_32x32x16_bf16 %[oa], v[188:191], v[164:167], %[oa]\n\t" \
    "v_cvt_pk_bf16_f32 v172, v152, v153\n\t" \
    "v_cvt_pk_bf16_f32 v173, v154, v155\n\t" \
    "s_cmp_lt_u32 %[rd], 3\n\t" \
    "s_cbranch_scc0 LD3_%=\n\t" \
    "s_add_u32 m0, m0, 4096\n\t" \
    "v_add_u32 v192, 4096, %[dv]\n\t" \
    "global_load_lds_dwordx4 v192, %[kvg] offset:0 sc1\n\t" \
    "LD3_%=:\n\t" \
    "v_cvt_pk_bf16_f32 v174, v156, v157\n\t" \
    "v_cvt_pk_bf16_f32 v175, v158, v159\n\t" \
    "v_exp_f32 v128, v128\n\t" \
    "v_exp_f32 v129, v129\n\t" \
    "v_mfma_f32_32x32x16_bf16 v[144:159], v[176:179], %[qb0], 0\n\t" \
    "v_exp_f32 v130, v130\n\t" \
    "v_exp_f32 v131, v131\n\t" \
    "v_exp_f32 v132, v132\n\t" \
    "v_exp_f32 v133, v133\n\t" \
    "v_exp_f32 v134, v134\n\t" \
    "v_exp_f32 v135, v135\n\t" \
    "v_mfma_f32_32x32x16_bf16 v[144:159], v[180:183], %[qb1], v[144:159]\n\t" \
    "v_cvt_pk_bf16_f32 v160, v128, v129\n\t" \
    "ds_read_b128 v[176:179], %[ak0] offset:12160\n\t" \
    "ds_read_b32 v180, %[ak1] offset:12160\n\t" \
    "v_cvt_pk_bf16_f32 v161, v130, v131\n\t" \
    "v_exp_f32 v136, v136\n\t" \
    "v_exp_f32 v137, v137\n\t" \
    "v_exp_f32 v138, v138\n\t" \
    "v_exp_f32 v139, v139\n\t" \
    "v_mfma_f32_32x32x16_bf16 %[ob], v[184:187], v[168:171], %[ob]\n\t" \
    "v_cvt_pk_bf16_f32 v162, v132, v133\n\t" \
    "v_cvt_pk_bf16_f32 v163, v134, v135\n\t" \
    "v_exp_f32 v140, v140\n\t" \
    "v_exp_f32 v141, v141\n\t" \
    "v_exp_f32 v142, v142\n\t" \
    "v_exp_f32 v143, v143\n\t" \
    "v_mfma_f32_32x32x16_bf16 %[ob], v[188:191], v[172:175], %[ob]\n\t" \
    "v_cvt_pk_bf16_f32 v164, v136, v137\n\t" \
    "ds_read_b128 v[184:187], %[av] offset:9728\n\t" \
    "ds_read_b128 v[188:191], %[av] offset:10368\n\t" \
    "v_cvt_pk_bf16_f32 v165, v138, v139\n\t" \
    "v_cvt_pk_bf16_f32 v166, v140, v141\n\t" \
    "v_cvt_pk_bf16_f32 v167, v142, v143\n\t" \
    "v_exp_f32 v144, v144\n\t" \
    "v_exp_f32 v145, v145\n\t" \
    "s_waitcnt lgkmcnt(2)\n\t" \
    "v_mfma_f32_32x32x16_bf16 v[128:143], v[176:179], %[qa0], 0\n\t" \
    "v_exp_f32 v146, v146\n\t" \
    "v_exp_f32 v147, v147\n\t" \
    "v_exp_f32 v148, v148\n\t" \
    "v_exp_f32 v149, v149\n\t" \
    "v_exp_f32 v150, v150\n\t" \
    "v_exp_f32 v151, v151\n\t" \
    "v_mfma_f32_32x32x16_bf16 v[128:143], v[180:183], %[qa1], v[128:143]\n\t" \
    "v_cvt_pk_bf16_f32 v168, v144, v145\n\t" \
    "v_cvt_pk_bf16_f32 v169, v146, v147\n\t" \
    "v_exp_f32 v152, v152\n\t" \
    "v_exp_f32 v153, v153\n\t" \
    "v_exp_f32 v154, v154\n\t" \
    "v_exp_f32 v155, v155\n\t" \
    "s_waitcnt lgkmcnt(0)\n\t" \
    "v_mfma_f32_32x32x16_bf16 %[oa], v[184:187], v[160:163], %[oa]\n\t" \
    "v_cvt_pk_bf16_f32 v170, v148, v149\n\t" \
    "v_cvt_pk_bf16_f32 v171, v150, v151\n\t" \
    "v_exp_f32 v156, v156\n\t" \
    "v_exp_f32 v157, v157\n\t" \
    "v_exp_f32 v158, v158\n\t" \
    "v_exp_f32 v159, v159\n\t" \
    "v_mfma_f32_32x32x16_bf16 %[oa], v[188:191], v[164:167], %[oa]\n\t" \
    "v_cvt_pk_bf16_f32 v172, v152, v153\n\t" \
    "v_cvt_pk_bf16_f32 v173, v154, v155\n\t" \
    "v_cvt_pk_bf16_f32 v174, v156, v157\n\t" \
    "v_cvt_pk_bf16_f32 v175, v158, v159\n\t" \
    "v_exp_f32 v128, v128\n\t" \
    "v_exp_f32 v129, v129\n\t" \
    "v_mfma_f32_32x32x16_bf16 v[144:159], v[176:179], %[qb0], 0\n\t" \
    "v_exp_f32 v130, v130\n\t" \
    "v_exp_f32 v131, v131\n\t" \
    "v_exp_f32 v132, v132\n\t" \
    "v_exp_f32 v133, v133\n\t" \
    "v_exp_f32 v134, v134\n\t" \
    "v_exp_f32 v135, v135\n\t" \
    "v_mfma_f32_32x32x16_bf16 v[144:159], v[180:183], %[qb1], v[144:159]\n\t" \
    "v_cvt_pk_bf16_f32 v160, v128, v129\n\t" \
    "ds_read_b128 v[176:179], %[ak0] offset:14592\n\t" \
    "ds_read_b32 v180, %[ak1] offset:14592\n\t" \
    "v_cvt_pk_bf16_f32 v161, v130, v131\n\t" \
    "v_exp_f32 v136, v136\n\t" \
    "v_exp_f32 v137, v137\n\t" \
    "v_exp_f32 v138, v138\n\t" \
    "v_exp_f32 v139, v139\n\t" \
    "v_mfma_f32_32x32x16_bf16 %[ob], v[184:187], v[168:171], %[ob]\n\t" \
    "v_cvt_pk_bf16_f32 v162, v132, v133\n\t" \
    "v_cvt_pk_bf16_f32 v163, v134, v135\n\t" \
    "v_exp_f32 v140, v140\n\t" \
    "v_exp_f32 v141, v141\n\t" \
    "v_exp_f32 v142, v142\n\t" \
    "v_exp_f32 v143, v143\n\t" \
    "v_mfma_f32_32x32x16_bf16 %[ob], v[188:191], v[172:175], %[ob]\n\t" \
    "v_cvt_pk_bf16_f32 v164, v136, v137\n\t" \
    "ds_read_b128 v[184:187], %[av] offset:12160\n\t" \
    "ds_read_b128 v[188:191], %[av] offset:12800\n\t" \
    "v_cvt_pk_bf16_f32 v165, v138, v139\n\t" \
    "v_cvt_pk_bf16_f32 v166, v140, v141\n\t" \
    "v_cvt_pk_bf16_f32 v167, v142, v143\n\t" \
    "v_exp_f32 v144, v144\n\t" \
    "v_exp_f32 v145, v145\n\t" \
    "s_waitcnt lgkmcnt(2)\n\t" \
    "v_mfma_f32_32x32x16_bf16 v[128:143], v[176:179], %[qa0], 0\n\t" \
    "v_exp_f32 v146, v146\n\t" \
    "v_exp_f32 v147, v147\n\t" \
    "v_exp_f32 v148, v148\n\t" \
    "v_exp_f32 v149, v149\n\t" \
    "v_exp_f32 v150, v150\n\t" \
    "v_exp_f32 v151, v151\n\t" \
    "v_mfma_f32_32x32x16_bf16 v[128:143], v[180:183], %[qa1], v[128:143]\n\t" \
    "v_cvt_pk_bf16_f32 v168, v144, v145\n\t" \
    "v_cvt_pk_bf16_f32 v169, v146, v147\n\t" \
    "v_exp_f32 v152, v152\n\t" \
    "v_exp_f32 v153, v153\n\t" \
    "v_exp_f32 v154, v154\n\t" \
    "v_exp_f32 v155, v155\n\t" \
    "s_waitcnt lgkmcnt(0)\n\t" \
    "v_mfma_f32_32x32x16_bf16 %[oa], v[184:187], v[160:163], %[oa]\n\t" \
    "v_cvt_pk_bf16_f32 v170, v148, v149\n\t" \
    "v_cvt_pk_bf16_f32 v171, v150, v151\n\t" \
    "v_exp_f32 v156, v156\n\t" \
    "v_exp_f32 v157, v157\n\t" \
    "v_exp_f32 v158, v158\n\t" \
    "v_exp_f32 v159, v159\n\t" \
    "v_mfma_f32_32x32x16_bf16 %[oa], v[188:191], v[164:167], %[oa]\n\t" \
    "v_cvt_pk_bf16_f32 v172, v152, v153\n\t" \
    "v_cvt_pk_bf16_f32 v173, v154, v155\n\t" \
    "v_cvt_pk_bf16_f32 v174, v156, v157\n\t" \
    "v_cvt_pk_bf16_f32 v175, v158, v159\n\t" \
    "v_exp_f32 v128, v128\n\t" \
    "v_exp_f32 v129, v129\n\t" \
    "v_mfma_f32_32x32x16_bf16 v[144:159], v[176:179], %[qb0], 0\n\t" \
    "v_exp_f32 v130, v130\n\t" \
    "v_exp_f32 v131, v131\n\t" \
    "v_exp_f32 v132, v132\n\t" \
    "v_exp_f32 v133, v133\n\t" \
    "v_exp_f32 v134, v134\n\t" \
    "v_exp_f32 v135, v135\n\t" \
    "v_mfma_f32_32x32x16_bf16 v[144:159], v[180:183], %[qb1], v[144:159]\n\t" \
    "v_cvt_pk_bf16_f32 v160, v128, v129\n\t" \
    "ds_read_b128 v[176:179], %[ak0] offset:17024\n\t" \
    "ds_read_b32 v180, %[ak1] offset:17024\n\t" \
    "v_cvt_pk_bf16_f32 v161, v130, v131\n\t" \
    "v_exp_f32 v136, v136\n\t" \
    "v_exp_f32 v137, v137\n\t" \
    "v_exp_f32 v138, v138\n\t" \
    "v_exp_f32 v139, v139\n\t" \
    "v_mfma_f32_32x32x16_bf16 %[ob], v[184:187], v[168:171], %[ob]\n\t" \
    "v_cvt_pk_bf16_f32 v162, v132, v133\n\t" \
    "v_cvt_pk_bf16_f32 v163, v134, v135\n\t" \
    "v_exp_f32 v140, v140\n\t" \
    "v_exp_f32 v141, v141\n\t" \
    "v_exp_f32 v142, v142\n\t" \
    "v_exp_f32 v143, v143\n\t" \
    "v_mfma_f32_32x32x16_bf16 %[ob], v[188:191], v[172:175], %[ob]\n\t" \
    "v_cvt_pk_bf16_f32 v164, v136, v137\n\t" \
    "ds_read_b128 v[184:187], %[av] offset:14592\n\t" \
    "ds_read_b128 v[188:191], %[av] offset:15232\n\t" \
    "v_cvt_pk_bf16_f32 v165, v138, v139\n\t" \
    "v_cvt_pk_bf16_f32 v166, v140, v141\n\t" \
    "v_cvt_pk_bf16_f32 v167, v142, v143\n\t" \
    "v_exp_f32 v144, v144\n\t" \
    "v_exp_f32 v145, v145\n\t" \
    "s_waitcnt lgkmcnt(2)\n\t" \
    "v_mfma_f32_32x32x16_bf16 v[128:143], v[176:179], %[qa0], 0\n\t" \
    "v_exp_f32 v146, v146\n\t" \
    "v_exp_f32 v147, v147\n\t" \
    "v_exp_f32 v148, v148\n\t" \
    "v_exp_f32 v149, v149\n\t" \
    "v_exp_f32 v150, v150\n\t" \
    "v_exp_f32 v151, v151\n\t" \
    "v_mfma_f32_32x32x16_bf16 v[128:143], v[180:183], %[qa1], v[128:143]\n\t" \
    "v_cvt_pk_bf16_f32 v168, v144, v145\n\t" \
    "v_cvt_pk_bf16_f32 v169, v146, v147\n\t" \
    "v_exp_f32 v152, v152\n\t" \
    "v_exp_f32 v153, v153\n\t" \
    "v_exp_f32 v154, v154\n\t" \
    "v_exp_f32 v155, v155\n\t" \
    "s_waitcnt lgkmcnt(0)\n\t" \
    "v_mfma_f32_32x32x16_bf16 %[oa], v[184:187], v[160:163], %[oa]\n\t" \
    "v_cvt_pk_bf16_f32 v170, v148, v149\n\t" \
    "v_cvt_pk_bf16_f32 v171, v150, v151\n\t" \
    "v_exp_f32 v156, v156\n\t" \
    "v_exp_f32 v157, v157\n\t" \
    "v_exp_f32 v158, v158\n\t" \
    "v_exp_f32 v159, v159\n\t" \
    "v_mfma_f32_32x32x16_bf16 %[oa], v[188:191], v[164:167], %[oa]\n\t" \
    "v_cvt_pk_bf16_f32 v172, v152, v153\n\t" \
    "v_cvt_pk_bf16_f32 v173, v154, v155\n\t" \
    "v_cvt_pk_bf16_f32 v174, v156, v157\n\t" \
    "v_cvt_pk_bf16_f32 v175, v158, v159\n\t" \
    "v_exp_f32 v128, v128\n\t" \
    "v_exp_f32 v129, v129\n\t" \
    "v_mfma_f32_32x32x16_bf16 v[144:159], v[176:179], %[qb0], 0\n\t" \
    "v_exp_f32 v130, v130\n\t" \
    "v_exp_f32 v131, v131\n\t" \
    "v_exp_f32 v132, v132\n\t" \
    "v_exp_f32 v133, v133\n\t" \
    "v_exp_f32 v134, v134\n\t" \
    "v_exp_f32 v135, v135\n\t" \
    "v_mfma_f32_32x32x16_bf16 v[144:159], v[180:183], %[qb1], v[144:159]\n\t" \
    "v_cvt_pk_bf16_f32 v160, v128, v129\n\t" \
    "s_waitcnt vmcnt(0)\n\t" \
    "s_barrier\n\t" \
    "ds_read_b128 v[176:179], %[ak0] offset:38912\n\t" \
    "ds_read_b32 v180, %[ak1] offset:38912\n\t" \
    "v_cvt_pk_bf16_f32 v161, v130, v131\n\t" \
    "v_exp_f32 v136, v136\n\t" \
    "v_exp_f32 v137, v137\n\t" \
    "v_exp_f32 v138, v138\n\t" \
    "v_exp_f32 v139, v139\n\t" \
    "v_mfma_f32_32x32x16_bf16 %[ob], v[184:187], v[168:171], %[ob]\n\t" \
    "v_cvt_pk_bf16_f32 v162, v132, v133\n\t" \
    "v_cvt_pk_bf16_f32 v163, v134, v135\n\t" \
    "v_exp_f32 v140, v140\n\t" \
    "v_exp_f32 v141, v141\n\t" \
    "v_exp_f32 v142, v142\n\t" \
    "v_exp_f32 v143, v143\n\t" \
    "v_mfma_f32_32x32x16_bf16 %[ob], v[188:191], v[172:175], %[ob]\n\t" \
    "v_cvt_pk_bf16_f32 v164, v136, v137\n\t" \
    "ds_read_b128 v[184:187], %[av] offset:17024\n\t" \
    "ds_read_b128 v[188:191], %[av] offset:17664\n\t" \
    "v_cvt_pk_bf16_f32 v165, v138, v139\n\t" \
    "v_cvt_pk_bf16_f32 v166, v140, v141\n\t" \
    "v_cvt_pk_bf16_f32 v167, v142, v143\n\t" \
    "v_exp_f32 v144, v144\n\t" \
    "v_exp_f32 v145, v145\n\t" \
    "s_waitcnt lgkmcnt(2)\n\t" \
    "v_mfma_f32_32x32x16_bf16 v[128:143], v[176:179], %[qa0], 0\n\t" \
    "v_exp_f32 v146, v146\n\t" \
    "v_exp_f32 v147, v147\n\t" \
    "v_exp_f32 v148, v148\n\t" \
    "v_exp_f32 v149, v149\n\t" \
    "v_exp_f32 v150, v150\n\t" \
    "v_exp_f32 v151, v151\n\t" \
    "v_mfma_f32_32x32x16_bf16 v[128:143], v[180:183], %[qa1], v[128:143]\n\t" \
    "v_cvt_pk_bf16_f32 v168, v144, v145\n\t" \
    "v_cvt_pk_bf16_f32 v169, v146, v147\n\t" \
    "v_exp_f32 v152, v152\n\t" \
    "v_exp_f32 v153, v153\n\t" \
    "v_exp_f32 v154, v154\n\t" \
    "v_exp_f32 v155, v155\n\t" \
    "s_waitcnt lgkmcnt(0)\n\t" \
    "v_mfma_f32_32x32x16_bf16 %[oa], v[184:187], v[160:163], %[oa]\n\t" \
    "v_cvt_pk_bf16_f32 v170, v148, v149\n\t" \
    "v_cvt_pk_bf16_f32 v171, v150, v151\n\t" \
    "v_exp_f32 v156, v156\n\t" \
    "v_exp_f32 v157, v157\n\t" \
    "v_exp_f32 v158, v158\n\t" \
    "v_exp_f32 v159, v159\n\t" \
    "v_mfma_f32_32x32x16_bf16 %[oa], v[188:191], v[164:167], %[oa]\n\t" \
    "v_cvt_pk_bf16_f32 v172, v152, v153\n\t" \
    "v_cvt_pk_bf16_f32 v173, v154, v155\n\t" \
    "v_cvt_pk_bf16_f32 v174, v156, v157\n\t" \
    "v_cvt_pk_bf16_f32 v175, v158, v159\n\t" \
    "v_add_u32 %[ak0], 38912, %[ak0]\n\t" \
    "v_add_u32 %[ak1], 38912, %[ak1]\n\t" \
    "v_add_u32 %[av], 38912, %[av]\n\t" \
    "v_add_u32 %[dv], 38912, %[dv]\n\t" \
    "s_add_u32 %[dl], %[dl], 38912\n\t" \
    "s_add_u32 %[rd], %[rd], 1\n\t" \
    "s_cmp_lt_u32 %[rd], 4\n\t" \
    "s_cbranch_scc1 LATTN_LOOP_%=\n\t" \
    "s_nop 1\n\t" \
    "v_mfma_f32_32x32x16_bf16 %[ob], v[184:187], v[168:171], %[ob]\n\t" \
    "v_mfma_f32_32x32x16_bf16 %[ob], v[188:191], v[172:175], %[ob]\n\t" \
    "s_waitcnt lgkmcnt(0)\n\t" \
    "s_mov_b32 m0, %[m0s]\n\t" \
    "s_nop 7\n\t" \
    "s_nop 7\n\t"
#define ATTN_CLOB "v128","v129","v130","v131","v132","v133","v134","v135","v136","v137","v138","v139","v140","v141","v142","v143","v144","v145","v146","v147","v148","v149","v150","v151","v152","v153","v154","v155","v156","v157","v158","v159","v160","v161","v162","v163","v164","v165","v166","v167","v168","v169","v170","v171","v172","v173","v174","v175","v176","v177","v178","v179","v180","v181","v182","v183","v184","v185","v186","v187","v188","v189","v190","v191","v192"

DI void attn_loop(f32x16& oa, f32x16& ob, u32x4 qa0, u32x4 qa1, u32x4 qb0, u32x4 qb1, unsigned ak0, unsigned ak1, unsigned av, unsigned kones,
                  const char* kvg, unsigned dv, unsigned dl) {
    unsigned rd, m0s;
    asm volatile(ATTN_ASM : [oa] "+v"(oa), [ob] "+v"(ob), [ak0] "+v"(ak0), [ak1] "+v"(ak1), [av] "+v"(av), [dv] "+v"(dv), [dl] "+s"(dl), [rd] "=&s"(rd), [m0s] "=&s"(m0s)
                 : [qa0] "v"(qa0), [qa1] "v"(qa1), [qb0] "v"(qb0), [qb1] "v"(qb1), [kones] "v"(kones), [kvg] "s"(kvg) : "memory", "scc", ATTN_CLOB);
}
DI float sumsq_q(u32x4 f0, u32x4 f1) { float s = sumsq8(f0, 0.f); const unsigned e = f1[0]; s = dot2(e, e, s); return red32(s, OpAdd()); }

DI void stage3(const Ctx& c, int layer, int grp, int m, f32x4 kv0, f32x4 kv1) {
    char* ws = c.ws;
    const int i = c.w & 3, kh = c.w >> 2, t = c.t, g = c.g;
    const unsigned kvo = O_KV + layer * KVS;
    const bool kvw = i == (m >> 1) && kh == 0, isv = m & 1;
    const unsigned stb = O_ST + (1 + 2 * layer) * STB;
    const Stats sto = stats_load(c, stb, 4 * grp + i, t);
    f32x4 ck0 = {0.f, 0.f, 0.f, 0.f}; float ck1 = 0.f, ck2 = 0.f;
    if (kvw) {
        const unsigned cb = blockIdx.x * 224 + 80;
        ck0 = gldc4(c.rs, (cb + 4 * g) * 4, O_CS); ck1 = gldc1(c.rs, (cb + 16) * 4, O_CS); ck2 = gldc1(c.rs, (cb + 17) * 4, O_CS);
    }
    f32x4 acc[5]; u32x4 xf[9];
    proj_load<9>(c, O_X1 + layer * XB + ((4 * grp + i) * 72 + 36 * kh) * 256, xf);
    f32x4 csq[3];
#pragma unroll
    for (int q = 0; q < 3; ++q) csq[q] = gldc4(c.rs, (blockIdx.x * 224 + (q < 2 ? 16 * (2 * kh + q) : 64) + 4 * g) * 4, O_CS);
    SBAR();
    float mean, rstd;
    stats_finish(sto, mean, rstd);
    if (kvw) {
        const int panel = 4 * grp + i, T = panel >> 1, sx = panel & 1;
        const unsigned tb = kvo + T * KVT;
        const unsigned a = pk2((kv0[0] - mean * ck0[0]) * rstd, (kv0[1] - mean * ck0[1]) * rstd), b = pk2((kv0[2] - mean * ck0[2]) * rstd, (kv0[3] - mean * ck0[3]) * rstd);
        const unsigned e = pk2((kv1[0] - mean * ck1) * rstd, (kv1[1] - mean * ck2) * rstd);
        if (!isv) {
            const auto s0 = __builtin_amdgcn_permlane16_swap(a, a, false, false), s1 = __builtin_amdgcn_permlane16_swap(b, b, false, false);
            if ((g & 1) == 0) gst16(c.rs, (u32x4){s0[0], s1[0], s0[1], s1[1]}, (g >> 1) * 512 + (16 * sx + t) * 16, tb);
            if (g == 0) gst4(c.rs, e, 1024 + (16 * sx + t) * 4, tb);
            float n2 = dot2(b, b, dot2(a, a, 0.f)); if (g == 0) n2 = dot2(e, e, n2);
            n2 = red_t(red_g(n2), OpMax());
            if (c.lane == 0) gst4(c.rs, __float_as_uint(n2), panel * 8, kvo + KVB);
        } else {
            char LAS* tr = c.sm + L_S3 + 24576;
            *(LAS u32x2*)(tr + t * 40 + g * 8) = (u32x2){a, b};
            if (g == 0) *(LAS u32x2*)(tr + t * 40 + 32) = (u32x2){e, 0u};
            const u32x2 y0 = ldstr(tr + (4 * g + (t >> 2)) * 40 + (t & 3) * 8);
            u32x2 y1 = ldstr(tr + (4 * g + (t >> 2)) * 40 + 32);
            if (t >= 2) { const unsigned cst = t == 2 ? 0x3f803f80u : 0u; y1 = (u32x2){cst, cst}; }
            const unsigned y00 = y0[0], y01 = y0[1], y10 = y1[0], y11 = y1[1];
            const auto p0 = __builtin_amdgcn_permlane32_swap(y00, y00, false, false), p1 = __builtin_amdgcn_permlane32_swap(y01, y01, false, false);
            const auto q0 = __builtin_amdgcn_permlane32_swap(y10, y10, false, false), q1 = __builtin_amdgcn_permlane32_swap(y11, y11, false, false);
            const unsigned vb = 1152 + (sx * 2 + g) * 320;
            if (g < 2) {
                gst16(c.rs, (u32x4){p0[0], p1[0], p0[1], p1[1]}, vb + t * 16, tb);
                if (t < 4) gst16(c.rs, (u32x4){q0[0], q1[0], q0[1], q1[1]}, vb + (16 + t) * 16, tb);
            }
            float vm = max3(max2(fabsf(bflo(a)), fabsf(bfhi(a))), max2(fabsf(bflo(b)), fabsf(bfhi(b))), g == 0 ? max2(fabsf(bflo(e)), fabsf(bfhi(e))) : 0.f);
            vm = red_t(red_g(vm, OpMax()), OpMax());
            if (c.lane == 0) gst4(c.rs, __float_as_uint(vm), panel * 8 + 4, kvo + KVB);
        }
        SBAR();
    }
    const int kcb = C_KV + layer * KV_LINES;
    unsigned arrived = 0; unsigned long long exsv;
    proj_gemm<9>(c, acc, xf);
    if (kvw) {
        WAIT_VM(0);
        asm volatile("s_mov_b64 %[sv], exec\n\ts_mov_b64 exec, 1\n\tglobal_atomic_add %[r], %[z], %[one], %[p] sc0\n\ts_mov_b64 exec, %[sv]"
                     : [r] "=&v"(arrived), [sv] "=&s"(exsv) : [z] "v"(0u), [one] "v"(1u), [p] "s"(cnt_ptr(ws, kcb + (blockIdx.x & 15))) : "memory");
    }
    f32x4 r[3];
    proj_reduce(c, L_S3, acc, r);
    if (kvw) {
        asm volatile("s_waitcnt vmcnt(0)" : "+v"(arrived) :: "memory");
        unsigned last = 0;
        if (c.lane == 0 && arrived == 15u) last = __hip_atomic_fetch_add(cnt_ptr(ws, kcb + 16), 1u, RLX_AGENT) == 15u;
        if (__builtin_amdgcn_readfirstlane(last) && c.lane < 32) __hip_atomic_store(cnt_ptr(ws, kcb + 17 + c.lane), 1u, RLX_AGENT);
    }
    {
        char LAS* row = c.sm + L_QS + (i * 16 + t) * 192;
        const float sc = rstd * QSCALE;
        unsigned p[3][2];
#pragma unroll
        for (int q = 0; q < 3; ++q) { p[q][0] = pk2((r[q][0] - mean * csq[q][0]) * sc, (r[q][1] - mean * csq[q][1]) * sc); p[q][1] = pk2((r[q][2] - mean * csq[q][2]) * sc, (r[q][3] - mean * csq[q][3]) * sc); }
        *(LAS u32x2*)(row + (2 * kh) * 48 + g * 8) = (u32x2){p[0][0], p[0][1]};
        sts4(row + (2 * kh + 1) * 48 + g * 8 + 4, p[1][0]); sts4(row + (2 * kh + 1) * 48 + g * 8 + 8, p[1][1]);
        if (kh == 0 && g < 2) { sts4(row + (2 * g) * 48 + 32, p[2][0]); sts4(row + (2 * g + 1) * 48, p[2][1]); }
    }
    if (c.w == 0) wait_counters(ws, C_KV + layer * KV_LINES + 17 + (blockIdx.x >> 3), 1, 1u, c.lane);
    WG_BARRIER();
    const int qp = c.w & 3, half = c.w >> 2, r32 = c.lane & 31, h = c.lane >> 5;
    u32x4 mx4;
    asm volatile("global_load_dwordx4 %0, %1, %2 sc1" : "=v"(mx4) : "v"(c.lane16), "s"(ws + kvo + KVB) : "memory");
    const unsigned dst0 = (c.w < 7 ? 5 * c.w : 33) * 1024;
    dma_run<5, true>(ws + kvo, c.lane16 + dst0, c.lds0 + dst0);
    u32x4 qa0, qa1, qb0, qb1;
    {
        const char LAS* qs = c.sm + L_QS + r32 * 192 + qp * 48;
        qa0 = lds16(qs + h * 16); qb0 = lds16(qs + 32 * 192 + h * 16);
        const unsigned ea = *(const LAS unsigned*)(qs + 32), eb = *(const LAS unsigned*)(qs + 32 * 192 + 32);
        qa1 = (u32x4){h ? 0u : ea, 0u, 0u, 0u}; qb1 = (u32x4){h ? 0u : eb, 0u, 0u, 0u};
    }
    asm volatile("s_waitcnt vmcnt(5)" : "+v"(mx4) :: "memory");
    bool unsafe;
    {
        const unsigned k0 = mx4[0], v0 = mx4[1], k1 = mx4[2], v1 = mx4[3];
        const float km = red_t(red_g(fmaxf(__uint_as_float(k0), __uint_as_float(k1)), OpMax()), OpMax());
        const float vm = red_t(red_g(fmaxf(__uint_as_float(v0), __uint_as_float(v1)), OpMax()), OpMax());
        const float qn = fmaxf(sumsq_q(qa0, qa1), sumsq_q(qb0, qb1));
        unsafe = !__all(qn * km <= SAFE_BOUND2 && vm <= SAFE_VMAX);
    }
    const unsigned kb = half * 8 * KVT;
    if (unsafe) {
        float ma = -3.0e38f, mb = -3.0e38f;
        const f32x16 z = {};
        for (int T = 0; T < 64; ++T) {
            const u32x4 k0 = gld16(c.rs, c.lane16, kvo + T * KVT);
            const u32x4 k1 = {gld4(c.rs, 1024 + r32 * 4, kvo + T * KVT), 0u, 0u, 0u};
            const f32x16 sa2 = mfma32(k1, qa1, mfma32(k0, qa0, z)), sb2 = mfma32(k1, qb1, mfma32(k0, qb0, z));
#pragma unroll
            for (int e = 0; e < 16; ++e) { ma = fmaxf(ma, sa2[e]); mb = fmaxf(mb, sb2[e]); }
        }
        ma = red32(ma, OpMax()); mb = red32(mb, OpMax());
        const float ah = bfhi(pk2(0.f, -ma)), bh = bfhi(pk2(0.f, -mb));
        if (h == 0) { qa1[1] = pk2(ah, -ma - ah); qb1[1] = pk2(bh, -mb - bh); }
    }
    WAIT_VM(0);
    WG_BARRIER();
    f32x16 oa = {}, ob = {};
    const unsigned zrow = 1152 + 19 * 16;
    attn_loop(oa, ob, qa0, qa1, qb0, qb1, c.lds0 + kb + c.lane16, c.lds0 + kb + (h ? zrow : 1024 + r32 * 4), c.lds0 + kb + 1152 + h * 320 + (r32 < 19 ? r32 : 19) * 16,
              (unsafe && !h) ? 0x3f803f80u : 0u, ws + kvo, c.lane16 + dst0 + 16 * KVT, c.lds0 + dst0 + 16 * KVT);
    WG_BARRIER();
    {
        char LAS* ex = c.sm + L_AE + c.w * 2048 + c.lane * 4;
        char LAS* lf = c.sm + L_AE + 16384 + ((half * 2) * 4 + qp) * 512 + r32 * 16;
#pragma unroll
        for (int e = 0; e < 8; ++e) stsf(ex + e * 256, half ? oa[e] : ob[e]);
        if (h == 0) { stsf4(lf, (f32x4){oa[8], oa[9], oa[10], 0.f}); stsf4(lf + 4 * 512, (f32x4){ob[8], ob[9], ob[10], 0.f}); }
    }
    WG_BARRIER();
    {
        const char LAS* ex = c.sm + L_AE + (c.w ^ 4) * 2048 + c.lane * 4;
        const char LAS* lf = c.sm + L_AE + 16384 + (half * 4) * 512 + r32 * 16;
        float v[8];
#pragma unroll
        for (int e = 0; e < 8; ++e) v[e] = (half ? ob[e] : oa[e]) + ldsf(ex + e * 256);
        const float l = ldsf(lf + qp * 512 + 8) + ldsf(lf + qp * 512 + 8 * 512 + 8);
        const float inv = 1.f / l;
        const auto s0 = __builtin_amdgcn_permlane32_swap(pk2(v[0] * inv, v[1] * inv), pk2(v[4] * inv, v[5] * inv), false, false);
        const auto s1 = __builtin_amdgcn_permlane32_swap(pk2(v[2] * inv, v[3] * inv), pk2(v[6] * inv, v[7] * inv), false, false);
        const int panel = 4 * grp + 2 * half + (r32 >> 4);
        const unsigned ob_ = O_OB + layer * XB;
        gst16(c.rs, (u32x4){s0[0], s1[0], s0[1], s1[1]}, (panel * 72 + 9 * m + 2 * qp + h) * 256 + (r32 & 15) * 16, ob_);
        if (qp == 0 && h == 0) {
            u32x4 e4;
#pragma unroll
            for (int hd = 0; hd < 4; ++hd) {
                const f32x4 a = ldsf4(lf + hd * 512), b = ldsf4(lf + hd * 512 + 8 * 512);
                const float iv = 1.f / (a[2] + b[2]);
                e4[hd] = pk2((a[0] + b[0]) * iv, (a[1] + b[1]) * iv);
            }
            gst16(c.rs, e4, (panel * 72 + 9 * m + 8) * 256 + (r32 & 15) * 16, ob_);
        }
    }
}

__global__ void __launch_bounds__(512, 2) main_kernel(char* ws, unsigned short* out) {
    __shared__ __attribute__((aligned(16))) char smem[LDS_BYTES];
    Ctx c;
    c.ws = ws; c.rs = __builtin_amdgcn_make_buffer_rsrc((void*)ws, 0, WS_USED, 0x00020000); c.sm = (char LAS*)smem;
    c.tid = threadIdx.x; c.lane = c.tid & 63; c.w = __builtin_amdgcn_readfirstlane(c.tid >> 6); c.t = c.lane & 15; c.g = c.lane >> 4; c.lane16 = c.lane * 16; c.lds0 = (unsigned)(uintptr_t)c.sm;
    const int b = blockIdx.x, xcd = b & 7, rr = b >> 3;
    const int grp = xcd * 4 + (rr >> 3), m = rr & 7;
    const int R = 4 * (xcd >> 1) + (rr >> 3), hp = 8 * (xcd & 1) + (rr & 7);
    Resid res;
    dma_image<126, 0, 18>(ws + O_WA1 + hp * WA1S, c.lds0, c.w, c.lane16);
    load_resid(c, grp, m, O_XP0, res);
    if (c.tid < 256 && (c.tid & 127) < 112) {
        const int part = c.tid >> 7, col = c.tid & 127;
        const unsigned base = part ? O_CSPA + (hp * 112 + col) * 4 : O_CSP + (m * 112 + col) * 4, step = part ? 16 * 448 : 8 * 448;
        float v[18], a = 0.f;
#pragma unroll
        for (int k = 0; k < 18; ++k) v[k] = gldc1(c.rs, base + k * step, 0);
#pragma unroll
        for (int k = 0; k < 18; ++k) a += v[k];
        ((float*)(ws + O_CS))[b * 224 + part * 112 + col] = a;
    }
    constexpr int NPRE = 8;
#define PUBLISH(NP, IMG, CNT, NREP) do { constexpr int KS_ = (NP + 6) / 7, PRE_ = NPRE < KS_ ? NPRE : KS_; \
        if (c.w == 0) { WAIT_VM(0); } else { dma_image<NP, 0, PRE_>(IMG, c.lds0, c.w, c.lane16); WAIT_VMN(PRE_); } \
        WG_BARRIER(); \
        if (c.tid < NREP) signal_counter(ws, CNT, c.tid); \
        dma_image<NP, PRE_, KS_>(IMG, c.lds0, c.w, c.lane16); } while (0)
    for (int layer = 0; layer < 2; ++layer) {
        if (layer == 1 && c.w == 0) wait_counters(ws, C_X2 + (R % 3) * 32 + (R > 0 ? 2 * R - 1 : 0), R > 0 ? 3 : 2, 8u, c.lane);
        WAIT_VM(0);
        WG_BARRIER();
        stage1(fresh(c), layer, opaque(R), opaque(hp), layer ? O_X2 : O_XP0, O_ST + 2 * layer * STB, O_OA + layer * OAB);
        PUBLISH(90, ws + O_WOA + m * WOAS, C_OA + layer * 16 + R, 1);
        dma_image<43, 0, 7>(ws + O_KVW + (m & 1) * KVWS, c.lds0 + WOAS, c.w, c.lane16);
        f32x4 kv0 = {0.f, 0.f, 0.f, 0.f}, kv1 = kv0;
        {
            const Ctx d = fresh(c); const int gq = opaque(grp), mq = opaque(m), i = d.w & 3, kh = d.w >> 2;
            const bool kvw = i == (mq >> 1);
            u32x4 xi[9];
            if (kvw) {
                if (layer == 1) wait_counters(ws, C_X2 + gq, 1, 8u, d.lane);
                proj_load<9>(d, (layer ? O_X2 : O_XP0) + ((4 * gq + i) * 72 + 36 * kh) * 256, xi);
            }
            if (d.w == 0) wait_counters(ws, C_OA + layer * 16 + (gq >> 1), 1, 16u, d.lane);
            WAIT_VM(0);
            WG_BARRIER();
            f32x4 acc[5], r[3]; u32x4 xf[10];
            if (kvw && kh == 1) { kv_input_gemm(d, xi, WOAS, kv0, kv1); SBAR(); }
            proj_load<10>(d, O_OA + layer * OAB + ((4 * gq + i) * 80 + 40 * kh) * 256, xf);
            SBAR();
            if (kvw) {
                if (kh == 0) { kv_input_gemm(d, xi, WOAS, kv0, kv1); SBAR(); }
                proj_gemm<10, 1>(d, acc, xf, WOAS + WKS, kv0, kv1);
                if (kh == 1) { stsf4(d.sm + L_SCR2 + 24576 + d.lane16, kv0); stsf4(d.sm + L_SCR2 + 25600 + d.lane16, kv1); }
            } else proj_gemm<10>(d, acc, xf);
            proj_reduce(d, L_SCR2, acc, r);
            if (kvw && kh == 0) { kv0 += ldsf4(d.sm + L_SCR2 + 24576 + d.lane16); kv1 += ldsf4(d.sm + L_SCR2 + 25600 + d.lane16); }
            proj_finish(d, gq, mq, r, res, O_X1 + layer * XB, O_ST + (1 + 2 * layer) * STB, false, out);
        }
        PUBLISH(81, ws + O_WQ + m * WQS, C_X1 + layer * 32 + grp, 1);
        if (c.w == 0) wait_counters(ws, C_X1 + layer * 32 + grp, 1, 8u, c.lane);
        WAIT_VM(0);
        WG_BARRIER();
        stage3(fresh(c), layer, opaque(grp), opaque(m), kv0, kv1);
        PUBLISH(81, ws + O_WOB + m * WQS, C_OB + layer * 32 + grp, 1);
        if (c.w == 0) wait_counters(ws, C_OB + layer * 32 + grp, 1, 8u, c.lane);
        WAIT_VM(0);
        WG_BARRIER();
        {
            const Ctx d = fresh(c); const int gq = opaque(grp), mq = opaque(m);
            f32x4 acc[5], r[3]; u32x4 xf[9];
            proj_load<9>(d, O_OB + layer * XB + ((4 * gq + (d.w & 3)) * 72 + 36 * (d.w >> 2)) * 256, xf);
            SBAR();
            proj_gemm<9>(d, acc, xf);
            proj_reduce(d, L_SCR, acc, r);
            proj_finish(d, gq, mq, r, res, O_X2, O_ST + 2 * STB, layer == 1, out);
        }
        if (layer == 0) PUBLISH(126, ws + O_WA1 + hp * WA1S, C_X2 + grp, 3);
    }
    asm volatile("Lmain_code_end:" ::: "memory");
}

extern "C" void kernel_launch(void* const* d_in, const int* in_sizes, int n_in, void* d_out, int out_size, void* d_ws, size_t ws_size, hipStream_t stream) {
    (void)in_sizes; (void)n_in; (void)out_size; (void)ws_size;
    prep_kernel<<<P_N, 256, 0, stream>>>((const unsigned short*)d_in[0], (const unsigned short*)d_in[1], (const unsigned short*)d_in[2], (const unsigned short*)d_in[4],
                                         (const unsigned short*)d_in[5], (const unsigned short*)d_in[6], (const unsigned short*)d_in[7], (char*)d_ws);
    main_kernel<<<256, 512, 0, stream>>>((char*)d_ws, (unsigned short*)d_out);
}
```
